```python
import torch

def _softmax(x, axis=-1):
    return torch.softmax(x, dim=axis)
def _gelu(x):
    return torch.nn.functional.gelu(x, approximate="tanh")
def _silu(x):
    return torch.nn.functional.silu(x)
def _sigmoid(x):
    return torch.sigmoid(x)
def _layernorm(x, axis=-1, eps=1e-5):
    m = x.mean(dim=axis, keepdim=True)
    v = x.var(dim=axis, keepdim=True, unbiased=False)
    return (x - m) / torch.sqrt(v + eps)
def _scatter_add(tgt, idx, src, axis=0):
    return tgt.clone().index_add_(axis, idx, src)
def _segment_sum(src, seg, n):
    out = torch.zeros((n,) + src.shape[1:], dtype=src.dtype)
    return out.index_add_(0, seg, src)
def _pad(x, pw):
    flat = tuple(v for pair in reversed(pw) for v in pair)
    return torch.nn.functional.pad(x, flat)

kernel_name = 'gated_mix__ssm_conv_s4__glu_gated_bilinear__prompt'

def setup_inputs():

    _w = (lambda a: (lambda t: t.to(torch.bfloat16) if t.is_floating_point() else t)(torch.as_tensor(a)))
    def _A_setup():
        B, L, D, N = 1, 2048, 576, 128
        A_re = -(lambda _x: torch.exp(torch.as_tensor(_x)))((lambda *s: torch.randn(*s, dtype=torch.bfloat16) * 3.2)(D, N))
        A_im = torch.pi * (lambda *s: torch.randn(*s, dtype=torch.bfloat16) * 0.05)(D, N)
        B_re = (lambda *s: torch.randn(*s, dtype=torch.bfloat16) * 0.1)(D, N)
        B_im = (lambda *s: torch.randn(*s, dtype=torch.bfloat16) * 0.1)(D, N)
        C_re = (lambda *s: torch.randn(*s, dtype=torch.bfloat16) * 0.1)(D, N)
        C_im = (lambda *s: torch.randn(*s, dtype=torch.bfloat16) * 0.1)(D, N)
        log_dt = (lambda *s: torch.randn(*s, dtype=torch.bfloat16) * 0.05)(D) * 0.5 - 4.0
        Dskip = (lambda *s: torch.randn(*s, dtype=torch.bfloat16) * 0.05)(D)
        return {
            "u": _w((lambda *s: torch.randn(*s, dtype=torch.bfloat16) * 0.05)(B, L, D)),
            "A_re": _w(A_re),
            "A_im": _w(A_im),
            "B_re": _w(B_re),
            "B_im": _w(B_im),
            "C_re": _w(C_re),
            "C_im": _w(C_im),
            "log_dt": _w(log_dt),
            "Dskip": _w(Dskip),
        }
    _A_in = _A_setup()

    def _B_setup():
        B, S, D, F = 1, 2048, 576, 4608
        return {
            "x": _w((lambda *s: torch.randn(*s, dtype=torch.bfloat16) * 0.05)(B, S, D)),
            "w_gate": _w((lambda *s: torch.randn(*s, dtype=torch.bfloat16) * 0.8)(D, F) * (D ** -0.5)),
            "w_up": _w((lambda *s: torch.randn(*s, dtype=torch.bfloat16) * 0.8)(D, F) * (D ** -0.5)),
            "w_down": _w((lambda *s: torch.randn(*s, dtype=torch.bfloat16) * 0.8)(F, D) * (F ** -0.5)),
        }
    _B_in = _B_setup()

    return {
        "A_u": _A_in["u"],
        "A_A_re": _A_in["A_re"],
        "A_A_im": _A_in["A_im"],
        "A_B_re": _A_in["B_re"],
        "A_B_im": _A_in["B_im"],
        "A_C_re": _A_in["C_re"],
        "A_C_im": _A_in["C_im"],
        "A_log_dt": _A_in["log_dt"],
        "A_Dskip": _A_in["Dskip"],
        "B_x": _B_in["x"],
        "B_w_gate": _B_in["w_gate"],
        "B_w_up": _B_in["w_up"],
        "B_w_down": _B_in["w_down"],
        "gate_w": _w((lambda *s: torch.randn(*s, dtype=torch.bfloat16) * 0.05)(576) * 16.0),
    }

def reference(A_u, A_A_re, A_A_im, A_B_re, A_B_im, A_C_re, A_C_im, A_log_dt, A_Dskip, B_x, B_w_gate, B_w_up, B_w_down, gate_w):
    def _A_ref(u, A_re, A_im, B_re, B_im, C_re, C_im, log_dt, Dskip):
        Bsz, L, D = u.shape
        dt = torch.exp(log_dt)[:, None]
        A = A_re + 1j * A_im
        Bc = B_re + 1j * B_im
        Cc = C_re + 1j * C_im
        logdA = dt * A
        dB = dt * Bc
        l = (lambda x, dt: x.to(dt))(torch.arange(L), A_re.dtype)
        P = torch.exp(logdA[:, :, None] * l[None, None, :])
        K = torch.einsum("dn,dnl,dn->dl", Cc, P, dB).real
        L2 = 2 * L
        Kp = _pad(K, ((0, 0), (0, L)))
        up = (lambda x, *d: x.permute(*d))(u, 0, 2, 1)
        up = _pad(up, ((0, 0), (0, 0), (0, L)))
        Kf = (lambda x, axes=(-2,-1): torch.fft.rfft2(x.float(), dim=axes))(Kp[:, None, :], axes=(-2, -1))
        Uf = (lambda x, axes=(-2,-1): torch.fft.rfft2(x.float(), dim=axes))(up[:, :, None, :], axes=(-2, -1))
        Yf = Uf * Kf[None, :, :, :]
        y = (lambda x, s, axes=(-2,-1): torch.fft.irfft2(x, s=s, dim=axes).to(torch.bfloat16))(Yf, (1, L2), axes=(-2, -1))[:, :, 0, :L]
        y = (lambda x, *d: x.permute(*d))(y, 0, 2, 1) + u * Dskip[None, None, :]
        return y

    def _B_ref(x, w_gate, w_up, w_down):
        g = torch.einsum("bsd,df->bsf", x, w_gate)
        u = torch.einsum("bsd,df->bsf", x, w_up)
        h = g * u
        return torch.einsum("bsf,fd->bsd", h, w_down)

    x = A_u
    g = _sigmoid((x * gate_w).sum(-1, keepdims=True))
    a = _A_ref(x, A_A_re, A_A_im, A_B_re, A_B_im, A_C_re, A_C_im, A_log_dt, A_Dskip)
    b = _B_ref(x, B_w_gate, B_w_up, B_w_down)
    return g * a + (1 - g) * b
```

```cpp
#include <hip/hip_runtime.h>
#include <hip/hip_bf16.h>
typedef unsigned short u16;
__device__ __forceinline__ float b2f(u16 v){ return __uint_as_float(((unsigned)v)<<16); }
__device__ __forceinline__ u16 f2b(float f){ __bf16 b=(__bf16)f; return __builtin_bit_cast(u16,b); }
__device__ __forceinline__ float rb(float f){ return b2f(f2b(f)); }
constexpr int L=2048,D=576,N=128,F=4608;

__global__ void kgen(const u16*Are,const u16*Aim,const u16*Bre,const u16*Bim,const u16*Cre,const u16*Cim,const u16*logdt,float*K){
  int d=blockIdx.x;
  float dt=rb(expf(b2f(logdt[d])));
  for(int l=threadIdx.x;l<L;l+=blockDim.x){
    float lf=rb((float)l);
    float s=0.f;
    for(int n=0;n<N;n++){
      int i=d*N+n;
      float ar=dt*b2f(Are[i]), ai=dt*b2f(Aim[i]);
      float br=dt*b2f(Bre[i]), bi=dt*b2f(Bim[i]);
      float cr=b2f(Cre[i]), ci=b2f(Cim[i]);
      float e=expf(ar*lf); float sn,cs; sincosf(ai*lf,&sn,&cs);
      float pr=e*cs, pi=e*sn;
      float wr=cr*br-ci*bi, wi=cr*bi+ci*br;
      s+=wr*pr-wi*pi;
    }
    K[d*L+l]=s;
  }
}
__global__ void convk(const u16*u,const float*K,const u16*Dskip,u16*a){
  int d=blockIdx.x; __shared__ float sk[L], su[L];
  for(int l=threadIdx.x;l<L;l+=blockDim.x){ sk[l]=K[d*L+l]; su[l]=b2f(u[l*D+d]);}
  __syncthreads();
  for(int t=threadIdx.x;t<L;t+=blockDim.x){
    float s=0.f; for(int j=0;j<=t;j++) s+=sk[j]*su[t-j];
    float y=rb(s); float sk2=rb(su[t]*b2f(Dskip[d]));
    a[t*D+d]=f2b(y+sk2);
  }
}
__global__ void g1(const u16*x,const u16*wg,const u16*wu,u16*h){
  int f=blockIdx.x*256+threadIdx.x; int r0=blockIdx.y*8;
  float g[8]={},uu[8]={};
  for(int k=0;k<D;k++){ float a=b2f(wg[k*F+f]), b=b2f(wu[k*F+f]);
    for(int i=0;i<8;i++){ float xv=b2f(x[(r0+i)*D+k]); g[i]+=xv*a; uu[i]+=xv*b; } }
  for(int i=0;i<8;i++) h[(size_t)(r0+i)*F+f]=f2b(rb(g[i])*rb(uu[i]));
}
__global__ void g2(const u16*x,const u16*h,const u16*wd,const u16*a,const u16*gw,u16*out){
  int dcol=blockIdx.x*64+threadIdx.x; int r0=blockIdx.y*8;
  float acc[8]={};
  for(int k=0;k<F;k++){ float w=b2f(wd[k*D+dcol]);
    for(int i=0;i<8;i++) acc[i]+=b2f(h[(size_t)(r0+i)*F+k])*w; }
  for(int i=0;i<8;i++){
    int r=r0+i; float s=0.f; for(int k=0;k<D;k++) s+=rb(b2f(x[r*D+k])*b2f(gw[k]));
    s=rb(s); float g=rb(1.f/(1.f+expf(-s)));
    float av=b2f(a[r*D+dcol]); float b=rb(acc[i]);
    float o=rb(g*av)+rb(rb(1.f-g)*b);
    out[r*D+dcol]=f2b(o);
  }
}
extern "C" void kernel_launch(void* const* d_in, const int* in_sizes, int n_in, void* d_out, int out_size, void* d_ws, size_t ws_size, hipStream_t stream) {
  const u16* u=(const u16*)d_in[0];
  float* K=(float*)d_ws; u16* a=(u16*)((char*)d_ws+ (size_t)D*L*4); u16* h=a+(size_t)L*D;
  kgen<<<D,256,0,stream>>>((const u16*)d_in[1],(const u16*)d_in[2],(const u16*)d_in[3],(const u16*)d_in[4],(const u16*)d_in[5],(const u16*)d_in[6],(const u16*)d_in[7],K);
  convk<<<D,256,0,stream>>>(u,K,(const u16*)d_in[8],a);
  g1<<<dim3(F/256,L/8),256,0,stream>>>(u,(const u16*)d_in[10],(const u16*)d_in[11],h);
  g2<<<dim3(D/64,L/8),64,0,stream>>>(u,h,(const u16*)d_in[12],a,(const u16*)d_in[13],(u16*)d_out);
}
```

```cpp
#include <hip/hip_runtime.h>
typedef unsigned short u16;
typedef unsigned int u32;
typedef short s16x4 __attribute__((ext_vector_type(4)));
typedef __bf16 bf16x8 __attribute__((ext_vector_type(8)));
typedef __bf16 bf16x2 __attribute__((ext_vector_type(2)));
typedef _Float16 f16x2 __attribute__((ext_vector_type(2)));
typedef _Float16 f16x8 __attribute__((ext_vector_type(8)));
typedef float f32x2 __attribute__((ext_vector_type(2)));
typedef float f32x4 __attribute__((ext_vector_type(4)));
typedef float f32x16 __attribute__((ext_vector_type(16)));
typedef u32 u32x2 __attribute__((ext_vector_type(2)));
typedef u32 u32x4 __attribute__((ext_vector_type(4)));
#define LAS __attribute__((address_space(3)))
#define GAS __attribute__((address_space(1)))

constexpr int L_ = 2048, D_ = 576, NS_ = 128, F_ = 4608;
constexpr size_t WS_H   = 0;
constexpr size_t WS_WF  = WS_H + (size_t)32 * 144 * 4096;
constexpr size_t WS_UT  = WS_WF + (size_t)8 * 144 * 4608;
constexpr size_t WS_AT  = WS_UT + (size_t)D_ * L_ * 2;
constexpr size_t WS_G   = WS_AT + (size_t)D_ * L_ * 2;
constexpr size_t WS_CNT = WS_G + (size_t)L_ * 4;
constexpr size_t WS_DBG = 48u << 20;

__device__ __forceinline__ float bf2f(u16 v) { return __uint_as_float(((u32)v) << 16); }
__device__ __forceinline__ u32 pk_bf16(float lo, float hi) {
  f32x2 v = {lo, hi}; bf16x2 b = __builtin_convertvector(v, bf16x2); return __builtin_bit_cast(u32, b);
}
__device__ __forceinline__ float lo_f(u32 p) { return __uint_as_float(p << 16); }
__device__ __forceinline__ float hi_f(u32 p) { return __uint_as_float(p & 0xffff0000u); }
__device__ __forceinline__ float rbf(float x) { return lo_f(pk_bf16(x, 0.f)); }

__device__ __forceinline__ void glds16(const void* g, u32 lds_byte, char* smem) {
  __builtin_amdgcn_global_load_lds((const GAS void*)g, (LAS void*)(smem + lds_byte), 16, 0, 0);
}
typedef __amdgpu_buffer_rsrc_t rsrc_t;
__device__ __forceinline__ rsrc_t mk_rsrc(const void* p, u32 bytes) {
  return __builtin_amdgcn_make_buffer_rsrc((void*)p, 0, bytes, 0x00020000);
}
__device__ __forceinline__ void blds16(rsrc_t r, u32 voff, u32 soff, u32 lds_byte, char* smem) {
  __builtin_amdgcn_raw_ptr_buffer_load_lds(r, (LAS void*)(smem + lds_byte), 16, voff, soff, 0, 0);
}
__device__ __forceinline__ s16x4 tr_read(char* smem, u32 off) {
  return __builtin_amdgcn_ds_read_tr16_b64_v4i16((LAS s16x4*)(smem + off));
}
#define WAITV0 asm volatile("s_waitcnt vmcnt(0)" ::: "memory")
#define WAITL0 asm volatile("s_waitcnt lgkmcnt(0)" ::: "memory")
#define BAR __builtin_amdgcn_s_barrier()
#define SCHED0 __builtin_amdgcn_sched_barrier(0)

#define DMA_EVERY 2
#define DMA_N0 4
#define DMA_N1 4
constexpr int X0_ = 0, XST = 32768, W0_ = 65536, WST = 36992, UOFF = 18432 + 128;
constexpr int K1_LDS = W0_ + 2 * WST;

template <int NT>
struct Frag { bf16x8 x[2]; bf16x8 w[NT]; };

template <int NT>
__device__ __forceinline__ void k1_load_frags(Frag<NT>& f, char* smem, u32 xaddr, u32 waddr, int stage, int s) {
#pragma unroll
  for (int xi = 0; xi < 2; ++xi)
    f.x[xi] = *(const LAS bf16x8*)(LAS char*)(smem + xaddr + stage * XST + xi * 4096);
#pragma unroll
  for (int T = 0; T < NT; ++T) {
    s16x4 a = tr_read(smem, waddr + stage * WST + (16 * s) * 288 + 32 * T);
    s16x4 b = tr_read(smem, waddr + stage * WST + (16 * s + 4) * 288 + 32 * T);
    typedef short s16x8 __attribute__((ext_vector_type(8)));
    s16x8 c = __builtin_shufflevector(a, b, 0, 1, 2, 3, 4, 5, 6, 7);
    f.w[T] = __builtin_bit_cast(bf16x8, c);
  }
}
template <int NT>
__device__ __forceinline__ void k1_mma(f32x16 (&acc)[NT][2], const Frag<NT>& f) {
#pragma unroll
  for (int T = 0; T < NT; ++T)
#pragma unroll
    for (int xi = 0; xi < 2; ++xi)
      acc[T][xi] = __builtin_amdgcn_mfma_f32_32x32x16_bf16(f.w[T], f.x[xi], acc[T][xi], 0, 0, 0);
}
template <int NT, int NDMA>
__device__ __forceinline__ void k1_interleave() {
#pragma unroll
  for (int i = 0; i < NT * 2; ++i) {
    __builtin_amdgcn_sched_group_barrier(0x8, 1, 0);
    if (i < 2) __builtin_amdgcn_sched_group_barrier(0x100, 2, 0); else __builtin_amdgcn_sched_group_barrier(0x100, 1, 0);
    if (i < NDMA) __builtin_amdgcn_sched_group_barrier(0x10, 1, 0);
  }
}

template <int NT, int WN>
__device__ __forceinline__ void k1_body(char* smem, const u16* __restrict__ x, const u16* __restrict__ wg, const u16* __restrict__ wu,
                                        const u16* __restrict__ gw, char* __restrict__ ws, int ib, int jb, int wave, int lane) {
  constexpr int NW = (WN == 0) ? 4 : 5;
  const int wm = wave & 3;
  u32 xoff[4], woff[NW];
#pragma unroll
  for (int ii = 0; ii < 4; ++ii) {
    int rr = lane >> 3, row = 32 * wave + 8 * ii + rr;
    int c = (lane & 7) ^ ((row >> 1) & 7);
    xoff[ii] = row * 1152 + c * 16;
  }
  const int wi0 = (WN == 0) ? 4 * wave : 16 + 5 * (wave - 4);
#pragma unroll
  for (int k = 0; k < NW; ++k) {
    int wi = wi0 + k; int gi = wi >= 18 ? wi - 18 : wi;
    int ci = 64 * gi + lane, row = ci / 18, w18 = ci - row * 18;
    int colb = (w18 < 16) ? (256 * jb + 16 * w18) : (8192 + 32 * jb + 16 * (w18 - 16));
    woff[k] = row * 9216 + colb;
  }
  const rsrc_t rx = mk_rsrc((const char*)x + (size_t)ib * 256 * 1152, 256 * 1152);
  const rsrc_t rg = mk_rsrc(wg, D_ * F_ * 2), ru = mk_rsrc(wu, D_ * F_ * 2);
  const int r32 = lane & 31, hh = lane >> 5;
  u32 xa[4];
#pragma unroll
  for (int s = 0; s < 4; ++s) xa[s] = X0_ + (64 * wm + r32) * 128 + (((2 * s + hh) ^ ((r32 >> 1) & 7)) * 16);
  const int g4 = lane >> 4;
  const u32 wa = W0_ + ((g4 & 1) ? UOFF : 0) + (8 * (g4 >> 1) + ((lane & 15) >> 2)) * 288 + (lane & 3) * 8 + (WN ? 5 * 32 : 0);

  f32x16 acc[NT][2];
#pragma unroll
  for (int T = 0; T < NT; ++T) { acc[T][0] = (f32x16)(0.f); acc[T][1] = (f32x16)(0.f); }
  Frag<NT> f0, f1;

  auto dma1 = [&](int t, int stage, int d) {
    if (d < 4) blds16(rx, xoff[d], t * 128, X0_ + stage * XST + (32 * wave + 8 * d) * 128, smem);
    else {
      const int k = d - 4; int wi = wi0 + k; bool up = wi >= 18; int gi = up ? wi - 18 : wi;
      blds16(up ? ru : rg, woff[k], t * 64 * 9216, W0_ + stage * WST + (up ? UOFF : 0) + gi * 1024, smem);
    }
  };
  auto sub = [&](Frag<NT>& cur, Frag<NT>& nxt, bool do_mma, int stage, int s, int t1, int d0, int nd) {
    typedef short s16x8 __attribute__((ext_vector_type(8)));
    s16x4 ta[NT], tb[NT];
    int di = 0;
#pragma unroll
    for (int i = 0; i < 2 * NT; ++i) {
      if (do_mma) acc[i >> 1][i & 1] = __builtin_amdgcn_mfma_f32_32x32x16_bf16(cur.w[i >> 1], cur.x[i & 1], acc[i >> 1][i & 1], 0, 0, 0);
      if (i == 0) {
        nxt.x[0] = *(const LAS bf16x8*)(LAS char*)(smem + xa[s] + stage * XST);
        nxt.x[1] = *(const LAS bf16x8*)(LAS char*)(smem + xa[s] + stage * XST + 4096);
      } else if (i == 1) {
        ta[0] = tr_read(smem, wa + stage * WST + (16 * s) * 288);
        tb[0] = tr_read(smem, wa + stage * WST + (16 * s + 4) * 288);
      } else {
        const int T = i >> 1;
        if ((i & 1) == 0) ta[T] = tr_read(smem, wa + stage * WST + (16 * s) * 288 + 32 * T);
        else              tb[T] = tr_read(smem, wa + stage * WST + (16 * s + 4) * 288 + 32 * T);
      }
      if (di < nd && (i % DMA_EVERY) == (WN ? 1 : 2) % DMA_EVERY) { dma1(t1, stage ^ 1, d0 + di); ++di; }
      SCHED0;
    }
#pragma unroll
    for (int k = 0; k < 9; ++k) if (di < nd) { dma1(t1, stage ^ 1, d0 + di); ++di; }
#pragma unroll
    for (int T = 0; T < NT; ++T) nxt.w[T] = __builtin_bit_cast(bf16x8, __builtin_shufflevector(ta[T], tb[T], 0, 1, 2, 3, 4, 5, 6, 7));
    SCHED0;
  };
  auto step = [&](int t, int stage, bool first, bool last) {
    WAITV0; WAITL0; BAR; SCHED0;
    constexpr int ND = 4 + NW;
    constexpr int N0 = DMA_N0, N1 = (ND - N0 < DMA_N1) ? ND - N0 : DMA_N1, N2 = ND - N0 - N1;
    sub(f1, f0, !first, stage, 0, t + 1, 0, last ? 0 : N0);
    sub(f0, f1, true, stage, 1, t + 1, N0, last ? 0 : N1);
    sub(f1, f0, true, stage, 2, t + 1, N0 + N1, last ? 0 : N2);
    sub(f0, f1, true, stage, 3, t + 1, 0, 0);
  };
#pragma unroll
  for (int p = 0; p < 4 + NW; ++p) dma1(0, 0, p);
  step(0, 0, true, false);
  for (int t = 1; t < 7; t += 2) { step(t, 1, false, false); step(t + 1, 0, false, false); }
  step(7, 1, false, false);
  step(8, 0, false, true);
  k1_mma<NT>(acc, f1);
  SCHED0;
  const int t2 = 511 - (wave * 64 + lane);
  const int grow = 8 * (int)blockIdx.x + wave;
  u32 uv[8];
  if (t2 < 288) {
#pragma unroll
    for (int i = 0; i < 8; ++i) uv[i] = *(const u32*)(x + (size_t)(256 * ib + 8 * jb + i) * D_ + 2 * t2);
  }
  u32x4 gx0 = *(const u32x4*)(x + (size_t)grow * D_ + lane * 8), gk0 = *(const u32x4*)(gw + lane * 8);
  u32x4 gx1 = (u32x4)(0u), gk1 = (u32x4)(0u);
  if (lane < 8) { gx1 = *(const u32x4*)(x + (size_t)grow * D_ + 512 + lane * 8); gk1 = *(const u32x4*)(gw + 512 + lane * 8); }
  SCHED0;

  char* hb = ws + WS_H + (size_t)(4 * ib + wm) * 144 * 4096 + ((r32 >> 4) * 1024 + (hh * 16 + (r32 & 15)) * 16);
#pragma unroll
  for (int T = 0; T < NT; ++T) {
    const int G = 9 * jb + T + (WN ? 5 : 0);
#pragma unroll
    for (int xi = 0; xi < 2; ++xi) {
      u32x4 o;
#pragma unroll
      for (int i = 0; i < 4; ++i) {
        u32 p0 = pk_bf16(acc[T][xi][2 * i], acc[T][xi][8 + 2 * i]);
        u32 p1 = pk_bf16(acc[T][xi][2 * i + 1], acc[T][xi][8 + 2 * i + 1]);
        o[i] = pk_bf16(lo_f(p0) * hi_f(p0), lo_f(p1) * hi_f(p1));
      }
      *(u32x4*)(hb + (size_t)(G >> 1) * 4096 + xi * 2048 + (G & 1) * 512) = o;
    }
  }
  SCHED0;
  if (t2 < 288) {
    u32x4 o0, o1;
#pragma unroll
    for (int k = 0; k < 4; ++k) {
      o0[k] = __builtin_amdgcn_perm(uv[2 * k + 1], uv[2 * k], 0x05040100u);
      o1[k] = __builtin_amdgcn_perm(uv[2 * k + 1], uv[2 * k], 0x07060302u);
    }
    char* ut = ws + WS_UT + (size_t)(2 * t2) * 4096 + (256 * ib + 8 * jb) * 2;
    *(u32x4*)ut = o0; *(u32x4*)(ut + 4096) = o1;
  }
  {
    float s = 0.f;
#pragma unroll
    for (int k = 0; k < 4; ++k) {
      s += rbf(lo_f(gx0[k]) * lo_f(gk0[k])) + rbf(hi_f(gx0[k]) * hi_f(gk0[k]));
      s += rbf(lo_f(gx1[k]) * lo_f(gk1[k])) + rbf(hi_f(gx1[k]) * hi_f(gk1[k]));
    }
#pragma unroll
    for (int o2 = 32; o2 > 0; o2 >>= 1) s += __shfl_xor(s, o2);
    s = rbf(s);
    if (lane == 0) ((float*)(ws + WS_G))[grow] = rbf(1.f / (1.f + expf(-s)));
    if (blockIdx.x == 0 && wave == 0 && lane < 8) *(u32*)(ws + WS_CNT + lane * 256) = 0u;
  }
}

__global__ __launch_bounds__(512, 2) void k1_gemm1(const u16* __restrict__ x, const u16* __restrict__ wg, const u16* __restrict__ wu,
                                                   const u16* __restrict__ wd, const u16* __restrict__ gw, char* __restrict__ ws) {
  extern __shared__ __attribute__((aligned(16))) char smem[];
  const int b = blockIdx.x, idx = b >> 3;
  const int ib = idx & 7, jb = 4 * (b & 7) + (idx >> 3);
  const int wave = __builtin_amdgcn_readfirstlane(threadIdx.x >> 6), lane = threadIdx.x & 63;
  if (wave < 4) k1_body<5, 0>(smem, x, wg, wu, gw, ws, ib, jb, wave, lane);
  else          k1_body<4, 1>(smem, x, wg, wu, gw, ws, ib, jb, wave, lane);
}

constexpr int K2_LDS = 74368;
constexpr int NCONV = 192;
constexpr u32 AUX_SC1 = 16;

__device__ __forceinline__ u32x4 bload16(rsrc_t r, u32 voff, u32 soff) {
  return __builtin_bit_cast(u32x4, __builtin_amdgcn_raw_buffer_load_b128(r, voff, soff, 0));
}
__device__ __forceinline__ int gbase(int G) {
  int jb = G / 9, T = G - 9 * jb;
  return T < 8 ? 128 * jb + 16 * T : 4096 + 16 * jb;
}

__device__ __forceinline__ void gemm2_role(char* smem, const u16* __restrict__ wd, char* __restrict__ ws, u16* __restrict__ out,
                                           int ib, int idx, int wave, int lane) {
  const int rowsub = idx & 3, cb = idx >> 2, rb64 = 4 * ib + rowsub;
  const rsrc_t rh = mk_rsrc(ws + WS_H + (size_t)rb64 * 144 * 4096, 144 * 4096);
  const rsrc_t rw = mk_rsrc((const char*)wd + cb * 144, F_ * D_ * 2 - cb * 144);
  const u32 ring = wave * 18432;
  u32 woff[9];
  int second[3];
#pragma unroll
  for (int k = 0; k < 9; ++k) {
    int ci = 64 * k + lane, row = ci / 9, cc = ci - 9 * row;
    woff[k] = (row & 15) * 1152 + cc * 16;
    if (k == 2 || k == 4 || k == 6) second[k / 2 - 1] = (row >> 4) - (k / 2 - 1);
  }
  const int q = lane >> 4, i16 = lane & 15;
  const u32 wa = ring + (16 * (q >> 1) + 4 * (q & 1) + (i16 >> 2)) * 144 + (i16 & 3) * 8;
  f32x4 acc[5][4];
#pragma unroll
  for (int a = 0; a < 5; ++a)
#pragma unroll
    for (int b = 0; b < 4; ++b) acc[a][b] = (f32x4)(0.f);
  u32x4 hreg[2][8];
  const u32 hv = lane * 16;

  auto dma = [&](int s, int stage) {
    const int b0 = gbase(4 * s) * 1152, b1 = gbase(4 * s + 1) * 1152, b2 = gbase(4 * s + 2) * 1152, b3 = gbase(4 * s + 3) * 1152;
    const u32 l = ring + stage * 9216;
    blds16(rw, woff[0], b0, l, smem);
    blds16(rw, woff[1], b0, l + 1024, smem);
    blds16(rw, woff[2] + (second[0] ? b1 : b0), 0, l + 2048, smem);
    blds16(rw, woff[3], b1, l + 3072, smem);
    blds16(rw, woff[4] + (second[1] ? b2 : b1), 0, l + 4096, smem);
    blds16(rw, woff[5], b2, l + 5120, smem);
    blds16(rw, woff[6] + (second[2] ? b3 : b2), 0, l + 6144, smem);
    blds16(rw, woff[7], b3, l + 7168, smem);
    blds16(rw, woff[8], b3, l + 8192, smem);
  };
  auto hload = [&](int s, int st, int kk) {
#pragma unroll
    for (int r = 0; r < 4; ++r) hreg[st][kk * 4 + r] = bload16(rh, hv + r * 1024, s * 8192 + kk * 4096);
  };
  auto wfrag = [&](bf16x8 (&w)[5], int stage, int kk) {
    typedef short s16x8 __attribute__((ext_vector_type(8)));
#pragma unroll
    for (int dt = 0; dt < 5; ++dt) {
      s16x4 a = tr_read(smem, wa + stage * 9216 + kk * 32 * 144 + dt * 32);
      s16x4 b = tr_read(smem, wa + stage * 9216 + kk * 32 * 144 + 8 * 144 + dt * 32);
      w[dt] = __builtin_bit_cast(bf16x8, __builtin_shufflevector(a, b, 0, 1, 2, 3, 4, 5, 6, 7));
    }
  };
  auto mma = [&](const bf16x8 (&w)[5], int st, int kk) {
#pragma unroll
    for (int r = 0; r < 4; ++r)
#pragma unroll
      for (int dt = 0; dt < 5; ++dt)
        acc[dt][r] = __builtin_amdgcn_mfma_f32_16x16x32_bf16(w[dt], __builtin_bit_cast(bf16x8, hreg[st][kk * 4 + r]), acc[dt][r], 0, 0, 0);
  };
  auto step = [&](int s, int st, int mode) {
    if (mode < 2) asm volatile("s_waitcnt vmcnt(25)" ::: "memory"); else asm volatile("s_waitcnt vmcnt(8)" ::: "memory");
    bf16x8 w0[5], w1[5];
    wfrag(w0, st, 0);
    wfrag(w1, st, 1);
    SCHED0;
    mma(w0, st, 0);
    SCHED0;
    WAITL0;
    if (mode == 0) { dma(s + 8, st); hload(s + 8, st, 0); }
    SCHED0;
    mma(w1, st, 1);
    SCHED0;
    if (mode == 0) hload(s + 8, st, 1);
    SCHED0;
  };
  dma(wave, 0); hload(wave, 0, 0); hload(wave, 0, 1);
  dma(wave + 4, 1); hload(wave + 4, 1, 0); hload(wave + 4, 1, 1);
  SCHED0;
  for (int i = 0; i < 16; i += 2) { step(wave + 4 * i, 0, 0); step(wave + 4 * i + 4, 1, 0); }
  step(wave + 64, 0, 1);
  step(wave + 68, 1, 2);

  const float gv = ((const float*)(ws + WS_G))[rb64 * 64 + 16 * wave + i16];
  WAITL0; BAR;
#pragma unroll
  for (int r = 0; r < 4; ++r) {
    if (r != wave) {
      const int slot = (wave - r - 1) & 3;
#pragma unroll
      for (int dt = 0; dt < 5; ++dt)
        *(LAS f32x4*)(LAS char*)(smem + ((r * 3 + slot) * 5 + dt) * 1024 + lane * 16) = acc[dt][r];
    }
  }
  f32x4 own[5];
#pragma unroll
  for (int dt = 0; dt < 5; ++dt) own[dt] = wave == 0 ? acc[dt][0] : wave == 1 ? acc[dt][1] : wave == 2 ? acc[dt][2] : acc[dt][3];
  {
    const u32* cnt = (const u32*)(ws + WS_CNT + cb * 256);
    for (int it = 0; it < (1 << 22); ++it) {
      if (__hip_atomic_load(cnt, __ATOMIC_RELAXED, __HIP_MEMORY_SCOPE_AGENT) >= NCONV / 8) break;
      __builtin_amdgcn_s_sleep(4);
    }
  }
  {
    const rsrc_t ra = mk_rsrc(ws + WS_AT + (size_t)cb * 72 * 4096 + rb64 * 128, 72 * 4096);
    const int tid = wave * 64 + lane;
    u32x4 v[3];
#pragma unroll
    for (int k = 0; k < 3; ++k) {
      int ci = tid + 256 * k;
      if (ci < 576) v[k] = __builtin_bit_cast(u32x4, __builtin_amdgcn_raw_buffer_load_b128(ra, (ci >> 3) * 4096 + (ci & 7) * 16, 0, AUX_SC1));
    }
#pragma unroll
    for (int k = 0; k < 3; ++k) {
      int ci = tid + 256 * k;
      if (ci < 576) *(LAS u32x4*)(LAS char*)(smem + 61440 + (ci >> 3) * 144 + (ci & 7) * 16) = v[k];
    }
  }
  WAITL0; BAR;
  u16* orow = out + (size_t)(rb64 * 64 + 16 * wave + i16) * D_ + cb * 72 + 4 * q;
  const float omg = rbf(1.f - gv);
#pragma unroll
  for (int dt = 0; dt < 5; ++dt) {
    f32x4 s = own[dt];
#pragma unroll
    for (int sl = 0; sl < 3; ++sl) s += *(const LAS f32x4*)(LAS char*)(smem + ((wave * 3 + sl) * 5 + dt) * 1024 + lane * 16);
    if (dt < 4 || q < 2) {
      float o[4];
#pragma unroll
      for (int i = 0; i < 4; ++i) {
        float av = bf2f(*(const LAS u16*)(LAS char*)(smem + 61440 + (16 * dt + 4 * q + i) * 144 + (16 * wave + i16) * 2));
        o[i] = rbf(gv * av) + rbf(omg * rbf(s[i]));
      }
      u32x2 pv = {pk_bf16(o[0], o[1]), pk_bf16(o[2], o[3])};
      *(u32x2*)(orow + 16 * dt) = pv;
    }
  }
}

constexpr int RS = 528, TABE = 41 * RS, CPS = 4160, RED = 33312, KV = 57888, US = 60544, USTR = 4608;
__device__ __forceinline__ f32x2 cmul(f32x2 a, f32x2 b) {
  f32x2 t = a * (f32x2){b.x, b.x};
  return __builtin_elementwise_fma((f32x2){-a.y, a.x}, (f32x2){b.y, b.y}, t);
}
__device__ __forceinline__ f32x2 cexp_(float ar, float air) {
  float e = __builtin_amdgcn_exp2f(ar * 1.44269504088896341f);
  float fr = __builtin_amdgcn_fractf(air);
  return (f32x2){e * __builtin_amdgcn_cosf(fr), e * __builtin_amdgcn_sinf(fr)};
}
__device__ __forceinline__ void st_h2(char* smem, u32 off, f32x2 v) {
  *(LAS f16x2*)(LAS char*)(smem + off) = __builtin_convertvector(v, f16x2);
}

__device__ __forceinline__ void conv_role(char* smem, const u16* __restrict__ are, const u16* __restrict__ aim, const u16* __restrict__ bre,
                                          const u16* __restrict__ bim, const u16* __restrict__ cre, const u16* __restrict__ cim,
                                          const u16* __restrict__ logdt, const u16* __restrict__ dskip, char* __restrict__ ws,
                                          int cw, int wave, int lane) {
  const int tid = wave * 64 + lane, n = tid & 127, half = wave >> 1;
  const int d0 = 3 * cw;
#pragma unroll
  for (int ch = 0; ch < 3; ++ch)
    glds16(ws + WS_UT + (size_t)(d0 + ch) * 4096 + tid * 16, US + ch * USTR + 480 + wave * 1024, smem);
  if (tid < 90) *(LAS u32x4*)(LAS char*)(smem + US + (tid / 30) * USTR + (tid % 30) * 16) = (u32x4)(0u);
  float par[3][6], wsc[3];
#pragma unroll
  for (int ch = 0; ch < 3; ++ch) {
    const int o = (d0 + ch) * NS_ + n;
    const float dt = rbf(expf(bf2f(logdt[d0 + ch])));
    par[ch][0] = dt * bf2f(are[o]); par[ch][1] = dt * bf2f(aim[o]);
    float br = dt * bf2f(bre[o]), bi = dt * bf2f(bim[o]), cr = bf2f(cre[o]), ci = bf2f(cim[o]);
    par[ch][2] = cr * br - ci * bi; par[ch][3] = cr * bi + ci * br;
    float m = fmaxf(fabsf(par[ch][2]), fabsf(par[ch][3]));
#pragma unroll
    for (int o2 = 32; o2 > 0; o2 >>= 1) m = fmaxf(m, __shfl_xor(m, o2));
    if (lane == 0) *(LAS float*)(LAS char*)(smem + KV + (ch * 4 + wave) * 4) = m;
  }
  u32 gidx[15];
  {
    const int J = 2040 - 8 * tid;
#pragma unroll
    for (int x = -7; x <= 7; ++x) {
      int j = J + x;
      u32 bits = pk_bf16((float)j, 0.f) & 0xffffu;
      int idx = j < 256 ? j : (int)bits - 17024;
      gidx[x + 7] = KV + (j < 0 ? 656 : (j > 2047 ? 656 : idx)) * 4;
    }
  }
  WAITV0; WAITL0; BAR;
#pragma unroll
  for (int ch = 0; ch < 3; ++ch) {
    float m = fmaxf(*(const LAS float*)(LAS char*)(smem + KV + (ch * 4) * 4), *(const LAS float*)(LAS char*)(smem + KV + (ch * 4 + 1) * 4));
    int e = m > 0.f ? __builtin_amdgcn_frexp_expf(m) : 14;
    e = e < -100 ? -100 : e;
    wsc[ch] = __builtin_ldexpf(1.f, e - 14);
    float sc = __builtin_ldexpf(1.f, 14 - e);
    par[ch][2] *= sc; par[ch][3] *= sc;
  }
  WAITL0; BAR;
  const int q = lane >> 4, i16 = lane & 15;
  for (int ch = 0; ch < 3; ++ch) {
    const float ar = ch == 0 ? par[0][0] : ch == 1 ? par[1][0] : par[2][0];
    const float ai = (ch == 0 ? par[0][1] : ch == 1 ? par[1][1] : par[2][1]) * 0.15915494309189535f;
    const f32x2 W = {ch == 0 ? par[0][2] : ch == 1 ? par[1][2] : par[2][2], ch == 0 ? par[0][3] : ch == 1 ? par[1][3] : par[2][3]};
    const float ksc = ch == 0 ? wsc[0] : ch == 1 ? wsc[1] : wsc[2];
    f32x2 p[12];
    p[0] = cexp_(ar, ai);            p[1] = cmul(p[0], p[0]); p[2] = cmul(p[1], p[1]); p[3] = cmul(p[2], p[2]);
    p[4] = cexp_(ar * 16.f, ai * 16.f);   p[5] = cmul(p[4], p[4]); p[6] = cmul(p[5], p[5]); p[7] = cmul(p[6], p[6]);
    if (half == 0) {
#pragma unroll
      for (int s = 0; s < 4; ++s) {
        f32x2 E[16];
        E[0] = (f32x2){1.f, 0.f}; E[1] = p[s]; E[2] = p[s + 1]; E[3] = cmul(E[2], E[1]); E[4] = p[s + 2];
#pragma unroll
        for (int k = 1; k < 4; ++k) E[4 + k] = cmul(E[k], p[s + 2]);
        E[8] = p[s + 3];
#pragma unroll
        for (int k = 1; k < 8; ++k) E[8 + k] = cmul(E[k], p[s + 3]);
#pragma unroll
        for (int b = 0; b < 16; ++b) st_h2(smem, TABE + (16 * s + b) * RS + n * 4, E[b]);
      }
    } else {
      p[8] = cexp_(ar * 256.f, ai * 256.f); p[9] = cmul(p[8], p[8]); p[10] = cmul(p[9], p[9]); p[11] = cmul(p[10], p[10]);
      f32x2 X[16];
      X[0] = W; X[1] = cmul(W, p[4]); X[2] = cmul(W, p[5]); X[3] = cmul(X[1], p[5]);
#pragma unroll
      for (int k = 0; k < 4; ++k) X[4 + k] = cmul(X[k], p[6]);
#pragma unroll
      for (int k = 0; k < 8; ++k) X[8 + k] = cmul(X[k], p[7]);
#pragma unroll
      for (int a = 0; a < 16; ++a) st_h2(smem, a * RS + n * 4, (f32x2){X[a].x, -X[a].y});
#pragma unroll
      for (int s = 1; s < 4; ++s) {
        f32x2 Y[8];
        Y[0] = cmul(W, p[s + 7]); Y[1] = cmul(Y[0], p[s + 4]); Y[2] = cmul(Y[0], p[s + 5]); Y[3] = cmul(Y[1], p[s + 5]);
#pragma unroll
        for (int k = 0; k < 4; ++k) Y[4 + k] = cmul(Y[k], p[s + 6]);
#pragma unroll
        for (int a = 0; a < 8; ++a) st_h2(smem, (16 + 8 * (s - 1) + a) * RS + n * 4, (f32x2){Y[a].x, -Y[a].y});
      }
      f32x2 Z = cmul(W, p[11]);
      st_h2(smem, 40 * RS + n * 4, (f32x2){Z.x, -Z.y});
    }
    WAITL0; BAR;
    {
      const int xrow = (wave == 0 ? 0 : 8 + 8 * wave) + i16;
      f32x4 kacc = (f32x4)(0.f);
#pragma unroll
      for (int kb = 0; kb < 8; ++kb) {
        f16x8 a = *(const LAS f16x8*)(LAS char*)(smem + xrow * RS + kb * 64 + q * 16);
        f16x8 b = *(const LAS f16x8*)(LAS char*)(smem + TABE + (16 * wave + i16) * RS + kb * 64 + q * 16);
        kacc = __builtin_amdgcn_mfma_f32_16x16x32_f16(a, b, kacc, 0, 0, 0);
      }
      const int nrow = wave == 0 ? 16 : wave == 3 ? 9 : 8, r0 = wave == 0 ? 0 : 8 + 8 * wave;
#pragma unroll
      for (int i = 0; i < 4; ++i)
        if (4 * q + i < nrow) *(LAS float*)(LAS char*)(smem + KV + ((r0 + 4 * q + i) * 16 + i16) * 4) = kacc[i] * ksc;
      if (tid == 0) *(LAS float*)(LAS char*)(smem + KV + 656 * 4) = 0.f;
    }
    WAITL0; BAR;
    {
      float V[15];
#pragma unroll
      for (int x = 0; x < 15; ++x) V[x] = *(const LAS float*)(LAS char*)(smem + gidx[x]);
      u32 P[15];
#pragma unroll
      for (int x = 1; x < 15; ++x) P[x] = pk_bf16(V[x], V[x - 1]);
#pragma unroll
      for (int sg = 0; sg < 8; ++sg) {
        u32x4 c = {P[sg + 7], P[sg + 5], P[sg + 3], P[sg + 1]};
        *(LAS u32x4*)(LAS char*)(smem + sg * CPS + (sg >= 4 ? 32 : 0) + tid * 16) = c;
      }
      if (tid < 8) *(LAS u32x4*)(LAS char*)(smem + tid * CPS + (tid >= 4 ? 32 : 0) + 4096) = (u32x4)(0u);
    }
    WAITL0; BAR;
    f32x4 Y[8];
#pragma unroll
    for (int i = 0; i < 8; ++i) Y[i] = (f32x4)(0.f);
    const u32 ub = US + ch * USTR + 480;
    {
      const int sg = i16 & 7;
      const u32 abase = sg * CPS + (sg >= 4 ? 32 : 0) + (2040 - 16 + 8 * q - 8 * (i16 >> 3)) * 2;
      const u32 bbase = ub + (16 * i16 - 16 + 8 * q) * 2;
#pragma unroll
      for (int cc = 0; cc < 2; ++cc) {
        const int c = 2 * wave + cc;
        bf16x8 A[8], B[8];
#pragma unroll
        for (int pp = 0; pp < 8; ++pp) {
          A[pp] = *(const LAS bf16x8*)(LAS char*)(smem + abase - (8 * pp + c) * 64);
          B[pp] = *(const LAS bf16x8*)(LAS char*)(smem + bbase + (8 * pp - c) * 64);
        }
#pragma unroll
        for (int k = 0; k < 8; ++k)
#pragma unroll
          for (int pp = 0; pp + k < 8; ++pp)
            Y[pp + k] = __builtin_amdgcn_mfma_f32_16x16x32_bf16(A[pp], B[k], Y[pp + k], 0, 0, 0);
      }
    }
#pragma unroll
    for (int i = 0; i < 8; ++i) {
      const int ow = i >> 1;
      if (ow != wave) {
        const int slot = (wave - ow - 1) & 3;
        *(LAS f32x4*)(LAS char*)(smem + RED + ((i * 3 + slot) * 1024) + lane * 16) = Y[i];
      }
    }
    WAITL0; BAR;
    {
      const float dsk = bf2f(dskip[d0 + ch]);
      const rsrc_t ra = mk_rsrc(ws + WS_AT + (size_t)(d0 + ch) * 4096, 4096);
#pragma unroll
      for (int ii = 0; ii < 2; ++ii) {
        const int i = 2 * wave + ii;
        f32x4 s = wave == 0 ? Y[ii] : wave == 1 ? Y[2 + ii] : wave == 2 ? Y[4 + ii] : Y[6 + ii];
#pragma unroll
        for (int sl = 0; sl < 3; ++sl) s += *(const LAS f32x4*)(LAS char*)(smem + RED + ((i * 3 + sl) * 1024) + lane * 16);
        const int t = 256 * i + 16 * i16 + 4 * q;
        u32x2 uu = *(const LAS u32x2*)(LAS char*)(smem + ub + t * 2);
        float a0 = rbf(s[0]) + rbf(lo_f(uu[0]) * dsk), a1 = rbf(s[1]) + rbf(hi_f(uu[0]) * dsk);
        float a2 = rbf(s[2]) + rbf(lo_f(uu[1]) * dsk), a3 = rbf(s[3]) + rbf(hi_f(uu[1]) * dsk);
        u32x2 pv = {pk_bf16(a0, a1), pk_bf16(a2, a3)};
        __builtin_amdgcn_raw_buffer_store_b64(pv, ra, t * 2, 0, AUX_SC1);
      }
    }
    WAITL0; BAR;
  }
  WAITV0; BAR;
  if (tid == 0) __hip_atomic_fetch_add((u32*)(ws + WS_CNT + (d0 / 72) * 256), 1u, __ATOMIC_RELAXED, __HIP_MEMORY_SCOPE_AGENT);
}

struct K2Args { const u16 *wd, *are, *aim, *bre, *bim, *cre, *cim, *logdt, *dskip; char* ws; u16* out; };

__global__ __launch_bounds__(256, 2) void k2_gemm2_conv(const u16* __restrict__ wd, const u16* __restrict__ are, const u16* __restrict__ aim,
    const u16* __restrict__ bre, const u16* __restrict__ bim, const u16* __restrict__ cre, const u16* __restrict__ cim,
    const u16* __restrict__ logdt, const u16* __restrict__ dskip, char* __restrict__ ws, u16* __restrict__ out) {
  extern __shared__ __attribute__((aligned(16))) char smem[];
  const int b = blockIdx.x;
  const int wave = __builtin_amdgcn_readfirstlane(threadIdx.x >> 6), lane = threadIdx.x & 63;
  if (b < NCONV) conv_role(smem, are, aim, bre, bim, cre, cim, logdt, dskip, ws, b, wave, lane);
  else { const int g = b - NCONV; gemm2_role(smem, wd, ws, out, g & 7, g >> 3, wave, lane); }
}

extern "C" void kernel_launch(void* const* d_in, const int* in_sizes, int n_in, void* d_out, int out_size, void* d_ws, size_t ws_size, hipStream_t stream) {
  (void)hipFuncSetAttribute((const void*)k1_gemm1, hipFuncAttributeMaxDynamicSharedMemorySize, K1_LDS);
  (void)hipFuncSetAttribute((const void*)k2_gemm2_conv, hipFuncAttributeMaxDynamicSharedMemorySize, K2_LDS);
  k1_gemm1<<<256, 512, K1_LDS, stream>>>((const u16*)d_in[0], (const u16*)d_in[10], (const u16*)d_in[11], (const u16*)d_in[12], (const u16*)d_in[13], (char*)d_ws);
  k2_gemm2_conv<<<NCONV + 256, 256, K2_LDS, stream>>>((const u16*)d_in[12], (const u16*)d_in[1], (const u16*)d_in[2], (const u16*)d_in[3], (const u16*)d_in[4],
      (const u16*)d_in[5], (const u16*)d_in[6], (const u16*)d_in[7], (const u16*)d_in[8], (char*)d_ws, (u16*)d_out);
}
```

```cpp
#include <hip/hip_runtime.h>
typedef unsigned short u16;
typedef unsigned int u32;
typedef short s16x4 __attribute__((ext_vector_type(4)));
typedef __bf16 bf16x8 __attribute__((ext_vector_type(8)));
typedef __bf16 bf16x2 __attribute__((ext_vector_type(2)));
typedef _Float16 f16x2 __attribute__((ext_vector_type(2)));
typedef _Float16 f16x8 __attribute__((ext_vector_type(8)));
typedef float f32x2 __attribute__((ext_vector_type(2)));
typedef float f32x4 __attribute__((ext_vector_type(4)));
typedef float f32x16 __attribute__((ext_vector_type(16)));
typedef u32 u32x2 __attribute__((ext_vector_type(2)));
typedef u32 u32x4 __attribute__((ext_vector_type(4)));
#define LAS __attribute__((address_space(3)))
#define GAS __attribute__((address_space(1)))

constexpr int L_ = 2048, D_ = 576, NS_ = 128, F_ = 4608;
constexpr size_t WS_H   = 0;
constexpr size_t WS_WF  = WS_H + (size_t)32 * 144 * 4096;
constexpr size_t WS_UT  = WS_WF + (size_t)8 * 144 * 4608;
constexpr size_t WS_AT  = WS_UT + (size_t)D_ * L_ * 2;
constexpr size_t WS_G   = WS_AT + (size_t)D_ * L_ * 2;
constexpr size_t WS_CNT = WS_G + (size_t)L_ * 4;
constexpr size_t WS_DBG = 48u << 20;

__device__ __forceinline__ float bf2f(u16 v) { return __uint_as_float(((u32)v) << 16); }
__device__ __forceinline__ u32 pk_bf16(float lo, float hi) {
  f32x2 v = {lo, hi}; bf16x2 b = __builtin_convertvector(v, bf16x2); return __builtin_bit_cast(u32, b);
}
__device__ __forceinline__ float lo_f(u32 p) { return __uint_as_float(p << 16); }
__device__ __forceinline__ float hi_f(u32 p) { return __uint_as_float(p & 0xffff0000u); }
__device__ __forceinline__ float rbf(float x) { return lo_f(pk_bf16(x, 0.f)); }

__device__ __forceinline__ void glds16(const void* g, u32 lds_byte, char* smem) {
  __builtin_amdgcn_global_load_lds((const GAS void*)g, (LAS void*)(smem + lds_byte), 16, 0, 0);
}
typedef __amdgpu_buffer_rsrc_t rsrc_t;
__device__ __forceinline__ rsrc_t mk_rsrc(const void* p, u32 bytes) {
  return __builtin_amdgcn_make_buffer_rsrc((void*)p, 0, bytes, 0x00020000);
}
__device__ __forceinline__ void blds16(rsrc_t r, u32 voff, u32 soff, u32 lds_byte, char* smem) {
  __builtin_amdgcn_raw_ptr_buffer_load_lds(r, (LAS void*)(smem + lds_byte), 16, voff, soff, 0, 0);
}
__device__ __forceinline__ s16x4 tr_read(char* smem, u32 off) {
  return __builtin_amdgcn_ds_read_tr16_b64_v4i16((LAS s16x4*)(smem + off));
}
#define WAITV0 asm volatile("s_waitcnt vmcnt(0)" ::: "memory")
#define WAITL0 asm volatile("s_waitcnt lgkmcnt(0)" ::: "memory")
#define BAR __builtin_amdgcn_s_barrier()
#define SCHED0 __builtin_amdgcn_sched_barrier(0)

#define DMA_EVERY 2
#define DMA_N0 4
#define DMA_N1 4
constexpr int X0_ = 0, XST = 32768, W0_ = 65536, WST = 36992, UOFF = 18432 + 128;
constexpr int WDN = X0_ + XST;
constexpr int UTS = W0_ + 2 * WST;
constexpr int GWL = UTS + 9216;
constexpr int K1_LDS = GWL + 1152;

template <int NT>
struct Frag { bf16x8 x[2]; bf16x8 w[NT]; };

template <int NT>
__device__ __forceinline__ void k1_load_frags(Frag<NT>& f, char* smem, u32 xaddr, u32 waddr, int stage, int s) {
#pragma unroll
  for (int xi = 0; xi < 2; ++xi)
    f.x[xi] = *(const LAS bf16x8*)(LAS char*)(smem + xaddr + stage * XST + xi * 4096);
#pragma unroll
  for (int T = 0; T < NT; ++T) {
    s16x4 a = tr_read(smem, waddr + stage * WST + (16 * s) * 288 + 32 * T);
    s16x4 b = tr_read(smem, waddr + stage * WST + (16 * s + 4) * 288 + 32 * T);
    typedef short s16x8 __attribute__((ext_vector_type(8)));
    s16x8 c = __builtin_shufflevector(a, b, 0, 1, 2, 3, 4, 5, 6, 7);
    f.w[T] = __builtin_bit_cast(bf16x8, c);
  }
}
template <int NT>
__device__ __forceinline__ void k1_mma(f32x16 (&acc)[NT][2], const Frag<NT>& f) {
#pragma unroll
  for (int T = 0; T < NT; ++T)
#pragma unroll
    for (int xi = 0; xi < 2; ++xi)
      acc[T][xi] = __builtin_amdgcn_mfma_f32_32x32x16_bf16(f.w[T], f.x[xi], acc[T][xi], 0, 0, 0);
}
template <int NT, int NDMA>
__device__ __forceinline__ void k1_interleave() {
#pragma unroll
  for (int i = 0; i < NT * 2; ++i) {
    __builtin_amdgcn_sched_group_barrier(0x8, 1, 0);
    if (i < 2) __builtin_amdgcn_sched_group_barrier(0x100, 2, 0); else __builtin_amdgcn_sched_group_barrier(0x100, 1, 0);
    if (i < NDMA) __builtin_amdgcn_sched_group_barrier(0x10, 1, 0);
  }
}

template <int NT, int WN>
__device__ __forceinline__ void k1_body(char* smem, const u16* __restrict__ x, const u16* __restrict__ wg, const u16* __restrict__ wu,
                                        const u16* __restrict__ wd, const u16* __restrict__ gw, char* __restrict__ ws, int ib, int jb, int wave, int lane) {
  constexpr int NW = (WN == 0) ? 4 : 5;
  const int wm = wave & 3;
  u32 xoff[4], woff[NW];
#pragma unroll
  for (int ii = 0; ii < 4; ++ii) {
    int rr = lane >> 3, row = 32 * wave + 8 * ii + rr;
    int c = (lane & 7) ^ ((row >> 1) & 7);
    xoff[ii] = row * 1152 + c * 16;
  }
  const int wi0 = (WN == 0) ? 4 * wave : 16 + 5 * (wave - 4);
#pragma unroll
  for (int k = 0; k < NW; ++k) {
    int wi = wi0 + k; int gi = wi >= 18 ? wi - 18 : wi;
    int ci = 64 * gi + lane, row = ci / 18, w18 = ci - row * 18;
    int colb = (w18 < 16) ? (256 * jb + 16 * w18) : (8192 + 32 * jb + 16 * (w18 - 16));
    woff[k] = row * 9216 + colb;
  }
  const rsrc_t rx = mk_rsrc((const char*)x + (size_t)ib * 256 * 1152, 256 * 1152);
  const rsrc_t rg = mk_rsrc(wg, D_ * F_ * 2), ru = mk_rsrc(wu, D_ * F_ * 2);
  const int r32 = lane & 31, hh = lane >> 5;
  u32 xa[4];
#pragma unroll
  for (int s = 0; s < 4; ++s) xa[s] = X0_ + (64 * wm + r32) * 128 + (((2 * s + hh) ^ ((r32 >> 1) & 7)) * 16);
  const int g4 = lane >> 4;
  const u32 wa = W0_ + ((g4 & 1) ? UOFF : 0) + (8 * (g4 >> 1) + ((lane & 15) >> 2)) * 288 + (lane & 3) * 8 + (WN ? 5 * 32 : 0);

  f32x16 acc[NT][2];
#pragma unroll
  for (int T = 0; T < NT; ++T) { acc[T][0] = (f32x16)(0.f); acc[T][1] = (f32x16)(0.f); }
  Frag<NT> f0, f1;
  const int lane_ = lane;

  auto dma1 = [&](int t, int stage, int d) {
    if (d < 4) blds16(rx, xoff[d], t * 128, X0_ + stage * XST + (32 * wave + 8 * d) * 128, smem);
    else {
      const int k = d - 4; int wi = wi0 + k; bool up = wi >= 18; int gi = up ? wi - 18 : wi;
      blds16(up ? ru : rg, woff[k], t * 64 * 9216, W0_ + stage * WST + (up ? UOFF : 0) + gi * 1024, smem);
    }
  };
  float gs0 = 0.f, gs1 = 0.f;
  u32 ux0 = 0, ux1 = 0, ugw = 0;
  const int urow = 8 * jb + 2 * (wave - 4);
  const u32 uaddr = X0_ + urow * 128 + (((lane >> 3) ^ ((urow >> 1) & 7)) << 4) + (lane & 7) * 2;
  auto sub = [&](Frag<NT>& cur, Frag<NT>& nxt, bool do_mma, int stage, int s, int t1, int d0, int nd) {
    typedef short s16x8 __attribute__((ext_vector_type(8)));
    s16x4 ta[NT], tb[NT];
    int di = 0;
#pragma unroll
    for (int i = 0; i < 2 * NT; ++i) {
      if (do_mma) acc[i >> 1][i & 1] = __builtin_amdgcn_mfma_f32_32x32x16_bf16(cur.w[i >> 1], cur.x[i & 1], acc[i >> 1][i & 1], 0, 0, 0);
      if (i == 0) {
        nxt.x[0] = *(const LAS bf16x8*)(LAS char*)(smem + xa[s] + stage * XST);
        nxt.x[1] = *(const LAS bf16x8*)(LAS char*)(smem + xa[s] + stage * XST + 4096);
      } else if (i == 1) {
        ta[0] = tr_read(smem, wa + stage * WST + (16 * s) * 288);
        tb[0] = tr_read(smem, wa + stage * WST + (16 * s + 4) * 288);
      } else {
        const int T = i >> 1;
        if ((i & 1) == 0) ta[T] = tr_read(smem, wa + stage * WST + (16 * s) * 288 + 32 * T);
        else              tb[T] = tr_read(smem, wa + stage * WST + (16 * s + 4) * 288 + 32 * T);
      }
      if (WN == 1) {
        if (s == 0 && i == 7) {
          ux0 = *(const LAS u16*)(LAS char*)(smem + uaddr + stage * XST);
          ux1 = *(const LAS u16*)(LAS char*)(smem + uaddr + stage * XST + 128);
          ugw = *(const LAS u16*)(LAS char*)(smem + GWL + ((t1 - 1) * 64 + lane) * 2);
        }
        if (s == 1 && i == 4) gs0 += rbf(__uint_as_float(ux0 << 16) * __uint_as_float(ugw << 16));
        if (s == 1 && i == 5) gs1 += rbf(__uint_as_float(ux1 << 16) * __uint_as_float(ugw << 16));
        if (s == 1 && i == 6) *(LAS u32*)(LAS char*)(smem + UTS + ((t1 - 1) * 64 + lane) * 16 + (wave - 4) * 4) = ux0 | (ux1 << 16);
      }
      if (di < nd && (i % DMA_EVERY) == (WN ? 1 : 2) % DMA_EVERY) { dma1(t1, stage ^ 1, d0 + di); ++di; }
      SCHED0;
    }
#pragma unroll
    for (int k = 0; k < 9; ++k) if (di < nd) { dma1(t1, stage ^ 1, d0 + di); ++di; }
#pragma unroll
    for (int T = 0; T < NT; ++T) nxt.w[T] = __builtin_bit_cast(bf16x8, __builtin_shufflevector(ta[T], tb[T], 0, 1, 2, 3, 4, 5, 6, 7));
    SCHED0;
  };
  auto wrelay = [&]() {
    int lane = lane_; asm volatile("" : "+v"(lane));
    const int g4 = lane >> 4, i16 = lane & 15, hh2 = g4 & 1;
#pragma unroll
    for (int r = 0; r < 3; ++r) {
      const int item = 2 * (wave + 8 * r) + (g4 >> 1);
      if (wave + 8 * r < 23) {
        const int it = item < 45 ? item : 44, T = it / 5, m = it - 5 * T;
        const u32 la = WDN + (16 * T + 4 * hh2 + (i16 >> 2)) * 144 + m * 32 + (i16 & 3) * 8;
        s16x4 a = tr_read(smem, la), b = tr_read(smem, la + 8 * 144);
        typedef short s16x8 __attribute__((ext_vector_type(8)));
        const u32x4 v = __builtin_bit_cast(u32x4, __builtin_shufflevector(a, b, 0, 1, 2, 3, 4, 5, 6, 7));
        const int G = 9 * jb + T, qq = 2 * (G & 1) + hh2;
        char* dst = ws + WS_WF + (size_t)(ib * 144 + (G >> 1)) * 4608 + (m < 4 ? m * 1024 + (qq * 16 + i16) * 16 : 4096 + (qq * 8 + i16) * 16);
        if (item < 45 && (m < 4 || i16 < 8)) *(u32x4*)dst = v;
      }
    }
  };
  u32 pfd = 0;
  u32 pfo;
  if (WN == 0) { const int l2 = (ib & 3) * 48 + wave * 12 + lane, row = l2 / 3, c = l2 - 3 * row; pfo = row * 9216 + (c < 2 ? 256 * jb + 128 * c : 8192 + 32 * jb); }
  else pfo = ((jb & 3) * 64 + (wave - 4) * 16 + lane) * 1152;
  const rsrc_t rpf = WN == 0 ? mk_rsrc(ib >= 4 ? wu : wg, D_ * F_ * 2) : rx;
  auto pf = [&](int tp) {
    if (tp <= 8) {
      const u32 so = WN == 0 ? tp * 64 * 9216 : tp * 128;
      if (lane < (WN == 0 ? 12 : 16)) asm volatile("buffer_load_dword %0, %1, %2, %3 offen" : "+v"(pfd) : "v"(pfo), "s"(rpf), "s"(so) : "memory");
    } else {
      int ln = lane_; asm volatile("" : "+v"(ln));
      const int li = min(wave * 36 + ln, 287), row = li >> 1, T = row >> 4;
      const u32 o2 = ((T < 8 ? 128 * jb + 16 * T : 4096 + 16 * jb) + (row & 15)) * 1152 + ib * 144 + (li & 1) * 128;
      const rsrc_t rd = mk_rsrc(wd, F_ * D_ * 2);
      if (ln < 36) asm volatile("buffer_load_dword %0, %1, %2, 0 offen" : "+v"(pfd) : "v"(min(o2, (u32)(F_ * D_ * 2 - 4))), "s"(rd) : "memory");
    }
  };
  auto wdn_dma = [&]() {
    int lane = lane_; asm volatile("" : "+v"(lane));
    const rsrc_t rd = mk_rsrc((const char*)wd + ib * 144, F_ * D_ * 2 - ib * 144);
#pragma unroll
    for (int r = 0; r < 3; ++r) {
      const int k = wave + 8 * r, ci = 64 * k + lane;
      if (k < 21) {
        if (ci < 1296) {
          const int row = ci / 9, cc = ci - 9 * row, T = row >> 4;
          const int f = (T < 8 ? 128 * jb + 16 * T : 4096 + 16 * jb) + (row & 15);
          blds16(rd, f * 1152 + cc * 16, 0, WDN + k * 1024, smem);
        }
      }
    }
  };
  auto step = [&](int t, int stage, bool first, bool last) {
    {
      int ne = 0;
      ne += first ? 2 : (t - 1 <= 6 ? 1 : 0);
      if (ne == 0) WAITV0;
      else if (ne == 1) asm volatile("s_waitcnt vmcnt(1)" ::: "memory");
      else if (ne == 2) asm volatile("s_waitcnt vmcnt(2)" ::: "memory");
      else if (ne == 3) asm volatile("s_waitcnt vmcnt(3)" ::: "memory");
      else asm volatile("s_waitcnt vmcnt(4)" ::: "memory");
    }
    WAITL0; BAR; SCHED0;
    if (last) { wdn_dma(); SCHED0; }
    constexpr int ND = 4 + NW;
    constexpr int N0 = DMA_N0, N1 = (ND - N0 < DMA_N1) ? ND - N0 : DMA_N1, N2 = ND - N0 - N1;
    sub(f1, f0, !first, stage, 0, t + 1, 0, last ? 0 : N0);
    sub(f0, f1, true, stage, 1, t + 1, N0, last ? 0 : N1);
    sub(f1, f0, true, stage, 2, t + 1, N0 + N1, last ? 0 : N2);
    SCHED0;
    if (t <= 6) pf(t + 3);
    SCHED0;
    sub(f0, f1, true, stage, 3, t + 1, 0, 0);
  };
  if (wave == 4) {
    const rsrc_t rgw = mk_rsrc(gw, D_ * 2);
    blds16(rgw, lane * 16, 0, GWL, smem);
    if (lane < 8) blds16(rgw, 1024 + lane * 16, 0, GWL + 1024, smem);
  }
#pragma unroll
  for (int p = 0; p < 4 + NW; ++p) dma1(0, 0, p);
pf(1); pf(2);
  step(0, 0, true, false);
  for (int t = 1; t < 7; t += 2) { step(t, 1, false, false); step(t + 1, 0, false, false); }
  step(7, 1, false, false);
  step(8, 0, false, true);
  k1_mma<NT>(acc, f1);
  WAITV0; WAITL0; BAR; SCHED0;
  asm volatile("" :: "v"(pfd));
  wrelay();
  {
    const int tid = wave * 64 + lane;
    char* ut = ws + WS_UT + (256 * ib + 8 * jb) * 2;
    *(u32x4*)(ut + (size_t)tid * 4096) = *(const LAS u32x4*)(LAS char*)(smem + UTS + tid * 16);
    if (tid < 64) *(u32x4*)(ut + (size_t)(512 + tid) * 4096) = *(const LAS u32x4*)(LAS char*)(smem + UTS + (512 + tid) * 16);
  }
  if (WN == 1) {
#pragma unroll
    for (int o2 = 32; o2 > 0; o2 >>= 1) { gs0 += __shfl_xor(gs0, o2); gs1 += __shfl_xor(gs1, o2); }
    const float v = rbf(lane == 0 ? gs0 : gs1);
    if (lane < 2) ((float*)(ws + WS_G))[256 * ib + urow + lane] = rbf(1.f / (1.f + expf(-v)));
    if (blockIdx.x == 0 && wave == 4 && lane < 8) *(u32*)(ws + WS_CNT + lane * 256) = 0u;
  }

  char* hb = ws + WS_H + (size_t)(4 * ib + wm) * 144 * 4096 + ((r32 >> 4) * 1024 + (hh * 16 + (r32 & 15)) * 16);
#pragma unroll
  for (int T = 0; T < NT; ++T) {
    const int G = 9 * jb + T + (WN ? 5 : 0);
#pragma unroll
    for (int xi = 0; xi < 2; ++xi) {
      u32x4 o;
#pragma unroll
      for (int i = 0; i < 4; ++i) {
        u32 p0 = pk_bf16(acc[T][xi][2 * i], acc[T][xi][8 + 2 * i]);
        u32 p1 = pk_bf16(acc[T][xi][2 * i + 1], acc[T][xi][8 + 2 * i + 1]);
        o[i] = pk_bf16(lo_f(p0) * hi_f(p0), lo_f(p1) * hi_f(p1));
      }
      *(u32x4*)(hb + (size_t)(G >> 1) * 4096 + xi * 2048 + (G & 1) * 512) = o;
    }
  }
}

__global__ __launch_bounds__(512, 2) void k1_gemm1(const u16* __restrict__ x, const u16* __restrict__ wg, const u16* __restrict__ wu,
                                                   const u16* __restrict__ wd, const u16* __restrict__ gw, char* __restrict__ ws) {
  extern __shared__ __attribute__((aligned(16))) char smem[];
  const int b = blockIdx.x, idx = b >> 3;
  const int ib = idx & 7, jb = 4 * (b & 7) + (idx >> 3);
  const int wave = __builtin_amdgcn_readfirstlane(threadIdx.x >> 6), lane = threadIdx.x & 63;
  if (wave < 4) k1_body<5, 0>(smem, x, wg, wu, wd, gw, ws, ib, jb, wave, lane);
  else          k1_body<4, 1>(smem, x, wg, wu, wd, gw, ws, ib, jb, wave, lane);
}

#define S2V(i)
#define S2R(i)
#define S2ID
constexpr int K2_LDS = 72672;
constexpr int NCONV = 192;
constexpr u32 AUX_SC1 = 16;

__device__ __forceinline__ u32x4 bload16(rsrc_t r, u32 voff, u32 soff) {
  return __builtin_bit_cast(u32x4, __builtin_amdgcn_raw_buffer_load_b128(r, voff, soff, 0));
}
__device__ __forceinline__ int gbase(int G) {
  int jb = G / 9, T = G - 9 * jb;
  return T < 8 ? 128 * jb + 16 * T : 4096 + 16 * jb;
}

#define G2D 3
__device__ __forceinline__ void gemm2_role(char* smem, char* __restrict__ ws, u16* __restrict__ out,
                                           int ib, int idx, int wave, int lane) {
  const int rowsub = idx & 3, cb = idx >> 2, rb64 = 4 * ib + rowsub;
  const rsrc_t rh = mk_rsrc(ws + WS_H + (size_t)rb64 * 144 * 4096, 144 * 4096);
  const rsrc_t rw = mk_rsrc(ws + WS_WF + (size_t)cb * 144 * 4608, 144 * 4608);
  const int q = lane >> 4, i16 = lane & 15;
  f32x4 acc[5][4];
#pragma unroll
  for (int a = 0; a < 5; ++a)
#pragma unroll
    for (int b = 0; b < 4; ++b) acc[a][b] = (f32x4)(0.f);
  u32x4 hb[G2D][4], wb[G2D][5];
  const u32 hv = lane * 16, hv4 = 4096 + (q * 8 + (i16 & 7)) * 16;
  auto ldh = [&](int j, int r, int ks) { hb[j][r] = bload16(rh, hv + r * 1024, ks * 4096); };
  auto ldw = [&](int j, int m, int ks) { wb[j][m] = m < 4 ? bload16(rw, hv + m * 1024, ks * 4608) : bload16(rw, hv4, ks * 4608); };
  auto ldall = [&](int j, int ks) {
#pragma unroll
    for (int r = 0; r < 3; ++r) ldh(j, r, ks);
#pragma unroll
    for (int m = 0; m < 5; ++m) ldw(j, m, ks);
    ldh(j, 3, ks);
    SCHED0;
  };
  auto kstep = [&](int j, int ksn, bool more) {
#pragma unroll
    for (int r = 0; r < 4; ++r) {
#pragma unroll
      for (int m = 0; m < 5; ++m) {
        acc[m][r] = __builtin_amdgcn_mfma_f32_16x16x32_bf16(__builtin_bit_cast(bf16x8, wb[j][m]), __builtin_bit_cast(bf16x8, hb[j][r]), acc[m][r], 0, 0, 0);
        if (r == 3 && more) { SCHED0; ldw(j, m, ksn); SCHED0; }
      }
      SCHED0;
      if (more) ldh(j, r, ksn);
      SCHED0;
    }
  };
  S2V(0); S2R(14); S2ID;
#pragma unroll
  for (int j = 0; j < G2D; ++j) ldall(j, wave + 4 * j);
  SCHED0;
  constexpr int NIT = 36 / G2D;
  const u32* cnt = (const u32*)(ws + WS_CNT + cb * 256);
  u32 early = 0;
  for (int it = 0; it < NIT - 1; ++it) {
    if (it == NIT - 2) early = __hip_atomic_load(cnt, __ATOMIC_RELAXED, __HIP_MEMORY_SCOPE_AGENT);
#pragma unroll
    for (int j = 0; j < G2D; ++j) kstep(j, wave + 4 * (G2D * (it + 1) + j), true);
    if (it == 0) S2V(1);
  }
  S2V(2);
  const int tid = wave * 64 + lane;
  u32x4 av[3];
  const float gv = ((const float*)(ws + WS_G))[rb64 * 64 + 16 * wave + i16];
  const rsrc_t ra = mk_rsrc(ws + WS_AT + (size_t)cb * 72 * 4096 + rb64 * 128, 72 * 4096);
  auto lda = [&]() {
#pragma unroll
    for (int k = 0; k < 3; ++k) {
      const int ci = min(tid + 256 * k, 575);
      av[k] = __builtin_bit_cast(u32x4, __builtin_amdgcn_raw_buffer_load_b128(ra, (ci >> 3) * 4096 + (ci & 7) * 16, 0, AUX_SC1));
    }
  };
  const bool ready = __builtin_amdgcn_readfirstlane(early) >= NCONV / 8;
  if (ready) lda();
  SCHED0;
  S2V(3);
#pragma unroll
  for (int j = 0; j < G2D; ++j) kstep(j, 0, false);
  S2V(4);

#pragma unroll
  for (int r = 0; r < 4; ++r) {
    if (r != wave) {
      const int slot = (wave - r - 1) & 3;
#pragma unroll
      for (int dt = 0; dt < 5; ++dt)
        *(LAS f32x4*)(LAS char*)(smem + ((r * 3 + slot) * 5 + dt) * 1024 + lane * 16) = acc[dt][r];
    }
  }
  f32x4 own[5];
#pragma unroll
  for (int dt = 0; dt < 5; ++dt) own[dt] = wave == 0 ? acc[dt][0] : wave == 1 ? acc[dt][1] : wave == 2 ? acc[dt][2] : acc[dt][3];
  if (!ready) {
    for (int it = 0; it < (1 << 22); ++it) {
      if (__hip_atomic_load(cnt, __ATOMIC_RELAXED, __HIP_MEMORY_SCOPE_AGENT) >= NCONV / 8) break;
      __builtin_amdgcn_s_sleep(2);
    }
    lda();
  }
#pragma unroll
  for (int k = 0; k < 3; ++k) {
    const int ci = tid + 256 * k;
    if (ci < 576) *(LAS u32x4*)(LAS char*)(smem + 61440 + (ci >> 3) * 144 + (ci & 7) * 16) = av[k];
  }
  S2V(5);
  WAITL0; BAR;
  u16* orow = out + (size_t)(rb64 * 64 + 16 * wave + i16) * D_ + cb * 72 + 4 * q;
  const float omg = rbf(1.f - gv);
#pragma unroll
  for (int dt = 0; dt < 5; ++dt) {
    f32x4 s = own[dt];
#pragma unroll
    for (int sl = 0; sl < 3; ++sl) s += *(const LAS f32x4*)(LAS char*)(smem + ((wave * 3 + sl) * 5 + dt) * 1024 + lane * 16);
    if (dt < 4 || q < 2) {
      float o[4];
#pragma unroll
      for (int i = 0; i < 4; ++i) {
        float av = bf2f(*(const LAS u16*)(LAS char*)(smem + 61440 + (16 * dt + 4 * q + i) * 144 + (16 * wave + i16) * 2));
        o[i] = rbf(gv * av) + rbf(omg * rbf(s[i]));
      }
      u32x2 pv = {pk_bf16(o[0], o[1]), pk_bf16(o[2], o[3])};
      *(u32x2*)(orow + 16 * dt) = pv;
    }
  }
  S2V(6);
  S2V(7); S2R(15);
}

constexpr int RS = 528, TABE = 41 * RS, CPS = 4160, CPY = 4 * CPS, KV = 3 * CPY, KVS = 2976, US = KV + 3 * KVS, USTR = 4608;
struct CP { f32x2 v, r; };
__device__ __forceinline__ CP mkcp(f32x2 p) { CP c; c.v = p; c.r = (f32x2){-p.y, p.x}; return c; }
__device__ __forceinline__ f32x2 cmul(f32x2 a, const CP& p) {
  return __builtin_elementwise_fma(p.r, (f32x2){a.y, a.y}, p.v * (f32x2){a.x, a.x});
}
__device__ __forceinline__ f32x2 cexp_(float ar, float air) {
  float e = __builtin_amdgcn_exp2f(ar * 1.44269504088896341f);
  float fr = __builtin_amdgcn_fractf(air);
  return (f32x2){e * __builtin_amdgcn_cosf(fr), e * __builtin_amdgcn_sinf(fr)};
}
__device__ __forceinline__ float wave_max_nonneg(float m) {
  int v = __float_as_int(m);
  v = max(v, __builtin_amdgcn_update_dpp(0, v, 0x111, 0xf, 0xf, true));
  v = max(v, __builtin_amdgcn_update_dpp(0, v, 0x112, 0xf, 0xf, true));
  v = max(v, __builtin_amdgcn_update_dpp(0, v, 0x114, 0xf, 0xf, true));
  v = max(v, __builtin_amdgcn_update_dpp(0, v, 0x118, 0xf, 0xf, true));
  const int a = __builtin_amdgcn_readlane(v, 15), b = __builtin_amdgcn_readlane(v, 31);
  const int c = __builtin_amdgcn_readlane(v, 47), d = __builtin_amdgcn_readlane(v, 63);
  return __int_as_float(max(max(a, b), max(c, d)));
}
__device__ __forceinline__ void st_h2(char* smem, u32 off, f32x2 v) {
  *(LAS f16x2*)(LAS char*)(smem + off) = __builtin_convertvector(v, f16x2);
}

__device__ __forceinline__ void conv_role(char* smem, const u16* __restrict__ are, const u16* __restrict__ aim, const u16* __restrict__ bre,
                                          const u16* __restrict__ bim, const u16* __restrict__ cre, const u16* __restrict__ cim,
                                          const u16* __restrict__ logdt, const u16* __restrict__ dskip, char* __restrict__ ws,
                                          int cw, int wave, int lane) {
  const int tid = wave * 64 + lane, n = tid & 127, half = wave >> 1;
  const int d0 = 3 * cw;
  S2V(0); S2R(14); S2ID;
  u16 raw[3][10], rdt[3], rds[3];
#pragma unroll
  for (int ch = 0; ch < 3; ++ch) {
    const int o = (d0 + ch) * NS_ + n, o2 = o ^ 64;
    rdt[ch] = logdt[d0 + ch]; rds[ch] = dskip[d0 + ch];
    raw[ch][0] = are[o]; raw[ch][1] = aim[o]; raw[ch][2] = bre[o]; raw[ch][3] = bim[o]; raw[ch][4] = cre[o]; raw[ch][5] = cim[o];
    raw[ch][6] = bre[o2]; raw[ch][7] = bim[o2]; raw[ch][8] = cre[o2]; raw[ch][9] = cim[o2];
  }
#pragma unroll
  for (int ch = 0; ch < 3; ++ch)
    glds16(ws + WS_UT + (size_t)(d0 + ch) * 4096 + tid * 16, US + ch * USTR + 480 + wave * 1024, smem);
  SCHED0;
  if (tid < 90) *(LAS u32x4*)(LAS char*)(smem + US + (tid / 30) * USTR + (tid % 30) * 16) = (u32x4)(0u);
  u32 gidx[11];
  {
    const int J = 2044 - 8 * tid;
#pragma unroll
    for (int x = -7; x <= 3; ++x) {
      int j = J + x;
      u32 bits = pk_bf16((float)j, 0.f) & 0xffffu;
      int idx = j < 256 ? j : (int)bits - 17024;
      gidx[x + 7] = KV + (j < 0 ? 740 : idx + (idx >> 3)) * 4;
    }
  }
  const int q = lane >> 4, i16 = lane & 15;
  u32 erow;
  {
    int s = wave, b = i16;
#pragma unroll
    for (int k = 0; k < 3; ++k) if (s > 0 && b < 8) { --s; b *= 2; }
    erow = TABE + (s == 0 ? b : 8 * s + b) * RS + q * 16;
  }
  SCHED0;
  float par[3][4], dsk[3], wsc[3];
#pragma unroll
  for (int ch = 0; ch < 3; ++ch) {
    const float dt = rbf(expf(bf2f(rdt[ch])));
    dsk[ch] = bf2f(rds[ch]);
    const float ar = dt * bf2f(raw[ch][0]), ai = dt * bf2f(raw[ch][1]) * 0.15915494309189535f;
    float br = dt * bf2f(raw[ch][2]), bi = dt * bf2f(raw[ch][3]), cr = bf2f(raw[ch][4]), ci = bf2f(raw[ch][5]);
    const float wr = __builtin_fmaf(cr, br, -(ci * bi)), wi = __builtin_fmaf(cr, bi, ci * br);
    br = dt * bf2f(raw[ch][6]); bi = dt * bf2f(raw[ch][7]); cr = bf2f(raw[ch][8]); ci = bf2f(raw[ch][9]);
    const float wr2 = __builtin_fmaf(cr, br, -(ci * bi)), wi2 = __builtin_fmaf(cr, bi, ci * br);
    float m = fmaxf(fmaxf(fabsf(wr), fabsf(wi)), fmaxf(fabsf(wr2), fabsf(wi2)));
    m = wave_max_nonneg(m);
    int e = m > 0.f ? __builtin_amdgcn_frexp_expf(m) : 14;
    e = e < -100 ? -100 : e;
    wsc[ch] = __builtin_ldexpf(1.f, e - 14);
    const float sc = __builtin_ldexpf(1.f, 14 - e);
    par[ch][0] = half ? ar * 16.f : ar; par[ch][1] = half ? ai * -16.f : ai;
    par[ch][2] = half ? wr * sc : 1.f; par[ch][3] = half ? -wi * sc : 0.f;
  }
  S2V(1);
  const u32 tabw = (half ? 0 : TABE) + n * 4;
  const u32 xrow = ((wave == 0 ? 0 : 8 + 8 * wave) + i16) * RS + q * 16;
  u32 kvw[4];
  {
    const int nrow = wave == 0 ? 16 : wave == 3 ? 9 : 8, r0 = wave == 0 ? 0 : 8 + 8 * wave;
#pragma unroll
    for (int i = 0; i < 4; ++i) kvw[i] = KV + (4 * q + i < nrow ? (r0 + 4 * q + i) * 18 + i16 + (i16 >> 3) : 741) * 4;
  }
  if (tid < 3) *(LAS float*)(LAS char*)(smem + KV + tid * KVS + 740 * 4) = 0.f;
  for (int ch = -1; ch < 3; ++ch) {
    f32x4 kacc = (f32x4)(0.f), kacc2 = (f32x4)(0.f);
    if (ch >= 0) {
      f16x8 ka[8], kb_[8];
#pragma unroll
      for (int kb = 0; kb < 8; ++kb) {
        ka[kb] = *(const LAS f16x8*)(LAS char*)(smem + xrow + kb * 64);
        kb_[kb] = *(const LAS f16x8*)(LAS char*)(smem + erow + kb * 64);
      }
      WAITL0; BAR;
      if (ch == 0) S2V(3);
#pragma unroll
      for (int kb = 0; kb < 8; kb += 2) {
        kacc = __builtin_amdgcn_mfma_f32_16x16x32_f16(ka[kb], kb_[kb], kacc, 0, 0, 0);
        kacc2 = __builtin_amdgcn_mfma_f32_16x16x32_f16(ka[kb + 1], kb_[kb + 1], kacc2, 0, 0, 0);
      }
      SCHED0;
    }
    if (ch < 2) {
      const int c1 = ch + 1;
      const float ar = c1 == 0 ? par[0][0] : c1 == 1 ? par[1][0] : par[2][0];
      const float ai = c1 == 0 ? par[0][1] : c1 == 1 ? par[1][1] : par[2][1];
      const f32x2 W = {c1 == 0 ? par[0][2] : c1 == 1 ? par[1][2] : par[2][2], c1 == 0 ? par[0][3] : c1 == 1 ? par[1][3] : par[2][3]};
      CP P[8];
      P[0] = mkcp(cexp_(ar, ai));
#pragma unroll
      for (int i = 1; i < 4; ++i) P[i] = mkcp(cmul(P[i - 1].v, P[i - 1]));
      P[4] = mkcp(cexp_(ar * 16.f, ai * 16.f));
#pragma unroll
      for (int i = 5; i < 8; ++i) P[i] = mkcp(cmul(P[i - 1].v, P[i - 1]));
      f32x2 X[16];
      X[0] = W; X[1] = cmul(W, P[0]); X[2] = cmul(W, P[1]); X[3] = cmul(X[1], P[1]);
#pragma unroll
      for (int k = 0; k < 4; ++k) X[4 + k] = cmul(X[k], P[2]);
#pragma unroll
      for (int k = 0; k < 8; ++k) X[8 + k] = cmul(X[k], P[3]);
#pragma unroll
      for (int a = 0; a < 16; ++a) st_h2(smem, tabw + a * RS, X[a]);
#pragma unroll
      for (int s = 1; s < 4; ++s) {
        f32x2 Y[8];
        Y[0] = cmul(W, P[s + 3]); Y[1] = cmul(Y[0], P[s]); Y[2] = cmul(Y[0], P[s + 1]); Y[3] = cmul(Y[1], P[s + 1]);
#pragma unroll
        for (int k = 0; k < 4; ++k) Y[4 + k] = cmul(Y[k], P[s + 2]);
#pragma unroll
        for (int a = 0; a < 8; ++a) st_h2(smem, tabw + (16 + 8 * (s - 1) + a) * RS, Y[a]);
      }
      st_h2(smem, tabw + 40 * RS, cmul(W, P[7]));
    }
    if (ch >= 0) {
      SCHED0;
      const float ksc = ch == 0 ? wsc[0] : ch == 1 ? wsc[1] : wsc[2];
      kacc += kacc2;
#pragma unroll
      for (int i = 0; i < 4; ++i) *(LAS float*)(LAS char*)(smem + kvw[i] + ch * KVS) = kacc[i] * ksc;
    }
    WAITL0; BAR;
    if (ch == -1) S2V(2);
    if (ch == 0) S2V(4);
  }
  S2V(5);
  {
    float V[3][11];
#pragma unroll
    for (int ch = 0; ch < 3; ++ch)
#pragma unroll
      for (int x = 0; x < 11; ++x) V[ch][x] = *(const LAS float*)(LAS char*)(smem + gidx[x] + ch * KVS);
    SCHED0;
#pragma unroll
    for (int ch = 0; ch < 3; ++ch) {
      u32 P[11];
#pragma unroll
      for (int x = 1; x < 11; ++x) P[x] = pk_bf16(V[ch][x], V[ch][x - 1]);
#pragma unroll
      for (int sg = 0; sg < 4; ++sg) {
        u32x4 c = {P[sg + 7], P[sg + 5], P[sg + 3], P[sg + 1]};
        *(LAS u32x4*)(LAS char*)(smem + ch * CPY + sg * CPS + tid * 16) = c;
      }
    }
  }
  if (tid < 24) *(LAS u32x4*)(LAS char*)(smem + (tid >> 1) * CPS + 4096 + (tid & 1) * 16) = (u32x4)(0u);
  WAITV0; WAITL0; BAR;
  S2V(6);
  auto finish = [&](const f32x4 s, int ch, int i) {
    const float dskc = ch == 0 ? dsk[0] : ch == 1 ? dsk[1] : dsk[2];
    const rsrc_t ra = mk_rsrc(ws + WS_AT + (size_t)(d0 + ch) * 4096, 4096);
    const int t = 256 * i + 16 * i16 + 4 * q;
    u32x2 uu = *(const LAS u32x2*)(LAS char*)(smem + US + ch * USTR + 480 + t * 2);
    float a0 = rbf(s[0]) + rbf(lo_f(uu[0]) * dskc), a1 = rbf(s[1]) + rbf(hi_f(uu[0]) * dskc);
    float a2 = rbf(s[2]) + rbf(lo_f(uu[1]) * dskc), a3 = rbf(s[3]) + rbf(hi_f(uu[1]) * dskc);
    u32x2 pv = {pk_bf16(a0, a1), pk_bf16(a2, a3)};
    __builtin_amdgcn_raw_buffer_store_b64(pv, ra, t * 2, 0, AUX_SC1);
  };
  const u32 abase0 = (i16 & 3) * CPS + (2044 - 16 + 8 * q - 4 * (i16 >> 2)) * 2;
  const u32 bbase0 = US + 480 + (16 * i16 - 16 + 8 * q) * 2;
  if (wave < 3) {
    constexpr int NTL = 8;
    f32x4 Y[NTL];
#pragma unroll
    for (int i = 0; i < NTL; ++i) Y[i] = (f32x4)(0.f);
    const u32 abase = abase0 + wave * CPY, bbase = bbase0 + wave * USTR;
    bf16x8 A0[NTL], B0[NTL], A1[NTL], B1[NTL];
    auto mm = [&](const bf16x8 (&A)[NTL], const bf16x8 (&B)[NTL], bf16x8 (&An)[NTL], bf16x8 (&Bn)[NTL], int cn, bool doit, bool more) {
      const u32 aa = abase - cn * 64, bb = bbase - cn * 64;
      u32x2 lo[NTL], hi[NTL];
      int idx = 0;
      WAITL0; SCHED0;
#pragma unroll
      for (int k = 0; k < NTL; ++k)
#pragma unroll
        for (int pp = 0; pp + k < NTL; ++pp) {
          if (doit) Y[pp + k] = __builtin_amdgcn_mfma_f32_16x16x32_bf16(A[pp], B[k], Y[pp + k], 0, 0, 0);
          if (more) {
            if (idx < NTL) Bn[idx] = *(const LAS bf16x8*)(LAS char*)(smem + bb + idx * 512);
            else if (idx < 2 * NTL) lo[idx - NTL] = *(const LAS u32x2*)(LAS char*)(smem + aa - (idx - NTL) * 512);
            else if (idx < 3 * NTL) hi[idx - 2 * NTL] = *(const LAS u32x2*)(LAS char*)(smem + aa - (idx - 2 * NTL) * 512 + 8);
          }
          ++idx;
          SCHED0;
        }
      if (more) {
#pragma unroll
        for (int pp = 0; pp < NTL; ++pp) An[pp] = __builtin_bit_cast(bf16x8, (u32x4){lo[pp][0], lo[pp][1], hi[pp][0], hi[pp][1]});
      }
    };
    mm(A1, B1, A0, B0, 0, false, true);
    for (int c = 0; c < 8; c += 2) {
      mm(A0, B0, A1, B1, c + 1, true, true);
      mm(A1, B1, A0, B0, c + 2, true, c + 2 < 8);
    }
    S2V(7);
#pragma unroll
    for (int i = 0; i < NTL; ++i) finish(Y[i], wave, i);
    S2V(8);
  }
  WAITV0; BAR;
  S2V(10); S2R(15);
  if (tid == 0) __hip_atomic_fetch_add((u32*)(ws + WS_CNT + (d0 / 72) * 256), 1u, __ATOMIC_RELAXED, __HIP_MEMORY_SCOPE_AGENT);
}

__global__ __launch_bounds__(256, 2) void k2_gemm2_conv(const u16* __restrict__ are, const u16* __restrict__ aim,
    const u16* __restrict__ bre, const u16* __restrict__ bim, const u16* __restrict__ cre, const u16* __restrict__ cim,
    const u16* __restrict__ logdt, const u16* __restrict__ dskip, char* __restrict__ ws, u16* __restrict__ out) {
  extern __shared__ __attribute__((aligned(16))) char smem[];
  const int b = blockIdx.x;
  const int wave = __builtin_amdgcn_readfirstlane(threadIdx.x >> 6), lane = threadIdx.x & 63;
  if (b < NCONV) conv_role(smem, are, aim, bre, bim, cre, cim, logdt, dskip, ws, b, wave, lane);
  else { const int g = b - NCONV; gemm2_role(smem, ws, out, g & 7, g >> 3, wave, lane); }
}

extern "C" void kernel_launch(void* const* d_in, const int* in_sizes, int n_in, void* d_out, int out_size, void* d_ws, size_t ws_size, hipStream_t stream) {
  (void)hipFuncSetAttribute((const void*)k1_gemm1, hipFuncAttributeMaxDynamicSharedMemorySize, K1_LDS);
  (void)hipFuncSetAttribute((const void*)k2_gemm2_conv, hipFuncAttributeMaxDynamicSharedMemorySize, K2_LDS);
  k1_gemm1<<<256, 512, K1_LDS, stream>>>((const u16*)d_in[0], (const u16*)d_in[10], (const u16*)d_in[11], (const u16*)d_in[12], (const u16*)d_in[13], (char*)d_ws);
  k2_gemm2_conv<<<NCONV + 256, 256, K2_LDS, stream>>>((const u16*)d_in[1], (const u16*)d_in[2], (const u16*)d_in[3], (const u16*)d_in[4],
      (const u16*)d_in[5], (const u16*)d_in[6], (const u16*)d_in[7], (const u16*)d_in[8], (char*)d_ws, (u16*)d_out);
}
```

```cpp
#include <hip/hip_runtime.h>
typedef unsigned short u16;
typedef unsigned int u32;
typedef short s16x4 __attribute__((ext_vector_type(4)));
typedef __bf16 bf16x8 __attribute__((ext_vector_type(8)));
typedef __bf16 bf16x2 __attribute__((ext_vector_type(2)));
typedef _Float16 f16x2 __attribute__((ext_vector_type(2)));
typedef _Float16 f16x8 __attribute__((ext_vector_type(8)));
typedef float f32x2 __attribute__((ext_vector_type(2)));
typedef float f32x4 __attribute__((ext_vector_type(4)));
typedef float f32x16 __attribute__((ext_vector_type(16)));
typedef u32 u32x2 __attribute__((ext_vector_type(2)));
typedef u32 u32x4 __attribute__((ext_vector_type(4)));
#define LAS __attribute__((address_space(3)))
#define GAS __attribute__((address_space(1)))

constexpr int L_ = 2048, D_ = 576, NS_ = 128, F_ = 4608;
constexpr size_t WS_H   = 0;
constexpr size_t WS_WF  = WS_H + (size_t)32 * 144 * 4096;
constexpr size_t WS_UT  = WS_WF + (size_t)8 * 144 * 4608;
constexpr size_t WS_AT  = WS_UT + (size_t)D_ * L_ * 2;
constexpr size_t WS_G   = WS_AT + (size_t)D_ * L_ * 2;
constexpr size_t WS_CNT = WS_G + (size_t)L_ * 4;
constexpr size_t WS_DBG = 48u << 20;

__device__ __forceinline__ float bf2f(u16 v) { return __uint_as_float(((u32)v) << 16); }
__device__ __forceinline__ u32 pk_bf16(float lo, float hi) {
  f32x2 v = {lo, hi}; bf16x2 b = __builtin_convertvector(v, bf16x2); return __builtin_bit_cast(u32, b);
}
__device__ __forceinline__ float lo_f(u32 p) { return __uint_as_float(p << 16); }
__device__ __forceinline__ float hi_f(u32 p) { return __uint_as_float(p & 0xffff0000u); }
__device__ __forceinline__ void mul_halves8(u32 (&q)[8]) {
  asm("v_dot2_f32_bf16 %0, %0, %8, 0\n\tv_dot2_f32_bf16 %1, %1, %9, 0\n\tv_dot2_f32_bf16 %2, %2, %10, 0\n\tv_dot2_f32_bf16 %3, %3, %11, 0\n\t"
      "v_dot2_f32_bf16 %4, %4, %12, 0\n\tv_dot2_f32_bf16 %5, %5, %13, 0\n\tv_dot2_f32_bf16 %6, %6, %14, 0\n\tv_dot2_f32_bf16 %7, %7, %15, 0\n\ts_nop 2"
      : "+v"(q[0]), "+v"(q[1]), "+v"(q[2]), "+v"(q[3]), "+v"(q[4]), "+v"(q[5]), "+v"(q[6]), "+v"(q[7])
      : "v"(q[0] >> 16), "v"(q[1] >> 16), "v"(q[2] >> 16), "v"(q[3] >> 16), "v"(q[4] >> 16), "v"(q[5] >> 16), "v"(q[6] >> 16), "v"(q[7] >> 16));
}
__device__ __forceinline__ float rbf(float x) { return lo_f(pk_bf16(x, 0.f)); }

__device__ __forceinline__ void glds16(const void* g, u32 lds_byte, char* smem) {
  __builtin_amdgcn_global_load_lds((const GAS void*)g, (LAS void*)(smem + lds_byte), 16, 0, 0);
}
typedef __amdgpu_buffer_rsrc_t rsrc_t;
__device__ __forceinline__ rsrc_t mk_rsrc(const void* p, u32 bytes) {
  return __builtin_amdgcn_make_buffer_rsrc((void*)p, 0, bytes, 0x00020000);
}
__device__ __forceinline__ void blds16(rsrc_t r, u32 voff, u32 soff, u32 lds_byte, char* smem) {
  __builtin_amdgcn_raw_ptr_buffer_load_lds(r, (LAS void*)(smem + lds_byte), 16, voff, soff, 0, 0);
}
__device__ __forceinline__ s16x4 tr_read(char* smem, u32 off) {
  return __builtin_amdgcn_ds_read_tr16_b64_v4i16((LAS s16x4*)(smem + off));
}
#define WAITV0 asm volatile("s_waitcnt vmcnt(0)" ::: "memory")
#define WAITL0 asm volatile("s_waitcnt lgkmcnt(0)" ::: "memory")
#define BAR __builtin_amdgcn_s_barrier()
#define SCHED0 __builtin_amdgcn_sched_barrier(0)

#define DMA_EVERY 2
#define DMA_N0 4
#define DMA_N1 4
constexpr int X0_ = 0, XST = 32768, W0_ = 65536, WST = 36992, UOFF = 18432 + 128;
constexpr int WDN = X0_ + XST;
constexpr int UTS = W0_ + 2 * WST;
constexpr int GWL = UTS + 9216;
constexpr int K1_LDS = GWL + 1152;

template <int NT>
struct Frag { bf16x8 x[2]; bf16x8 w[NT]; };

template <int NT>
__device__ __forceinline__ void k1_load_frags(Frag<NT>& f, char* smem, u32 xaddr, u32 waddr, int stage, int s) {
#pragma unroll
  for (int xi = 0; xi < 2; ++xi)
    f.x[xi] = *(const LAS bf16x8*)(LAS char*)(smem + xaddr + stage * XST + xi * 4096);
#pragma unroll
  for (int T = 0; T < NT; ++T) {
    s16x4 a = tr_read(smem, waddr + stage * WST + (16 * s) * 288 + 32 * T);
    s16x4 b = tr_read(smem, waddr + stage * WST + (16 * s + 4) * 288 + 32 * T);
    typedef short s16x8 __attribute__((ext_vector_type(8)));
    s16x8 c = __builtin_shufflevector(a, b, 0, 1, 2, 3, 4, 5, 6, 7);
    f.w[T] = __builtin_bit_cast(bf16x8, c);
  }
}
template <int NT>
__device__ __forceinline__ void k1_mma(f32x16 (&acc)[NT][2], const Frag<NT>& f) {
#pragma unroll
  for (int T = 0; T < NT; ++T)
#pragma unroll
    for (int xi = 0; xi < 2; ++xi)
      acc[T][xi] = __builtin_amdgcn_mfma_f32_32x32x16_bf16(f.w[T], f.x[xi], acc[T][xi], 0, 0, 0);
}
template <int NT, int NDMA>
__device__ __forceinline__ void k1_interleave() {
#pragma unroll
  for (int i = 0; i < NT * 2; ++i) {
    __builtin_amdgcn_sched_group_barrier(0x8, 1, 0);
    if (i < 2) __builtin_amdgcn_sched_group_barrier(0x100, 2, 0); else __builtin_amdgcn_sched_group_barrier(0x100, 1, 0);
    if (i < NDMA) __builtin_amdgcn_sched_group_barrier(0x10, 1, 0);
  }
}

template <int NT, int WN>
__device__ __forceinline__ void k1_body(char* smem, const u16* __restrict__ x, const u16* __restrict__ wg, const u16* __restrict__ wu,
                                        const u16* __restrict__ wd, const u16* __restrict__ gw, char* __restrict__ ws, int ib, int jb, int wave, int lane,
                                        const u16* p0, const u16* p1, const u16* p2, const u16* p3, const u16* p4, const u16* p5) {
  constexpr int NW = (WN == 0) ? 4 : 5;
  const int wm = wave & 3;
  u32 xoff[4], woff[NW];
#pragma unroll
  for (int ii = 0; ii < 4; ++ii) {
    int rr = lane >> 3, row = 32 * wave + 8 * ii + rr;
    int c = (lane & 7) ^ ((row >> 1) & 7);
    xoff[ii] = row * 1152 + c * 16;
  }
  const int wi0 = (WN == 0) ? 4 * wave : 16 + 5 * (wave - 4);
#pragma unroll
  for (int k = 0; k < NW; ++k) {
    int wi = wi0 + k; int gi = wi >= 18 ? wi - 18 : wi;
    int ci = 64 * gi + lane, row = ci / 18, w18 = ci - row * 18;
    int colb = (w18 < 16) ? (256 * jb + 16 * w18) : (8192 + 32 * jb + 16 * (w18 - 16));
    woff[k] = row * 9216 + colb;
  }
  const rsrc_t rx = mk_rsrc((const char*)x + (size_t)ib * 256 * 1152, 256 * 1152);
  const rsrc_t rg = mk_rsrc(wg, D_ * F_ * 2), ru = mk_rsrc(wu, D_ * F_ * 2);
  const int r32 = lane & 31, hh = lane >> 5;
  u32 xa[4];
#pragma unroll
  for (int s = 0; s < 4; ++s) xa[s] = X0_ + (64 * wm + r32) * 128 + (((2 * s + hh) ^ ((r32 >> 1) & 7)) * 16);
  const int g4 = lane >> 4;
  const u32 wa = W0_ + ((g4 & 1) ? UOFF : 0) + (8 * (g4 >> 1) + ((lane & 15) >> 2)) * 288 + (lane & 3) * 8 + (WN ? 5 * 32 : 0);

  f32x16 acc[NT][2];
#pragma unroll
  for (int T = 0; T < NT; ++T) { acc[T][0] = (f32x16)(0.f); acc[T][1] = (f32x16)(0.f); }
  Frag<NT> f0, f1;
  const int lane_ = lane;

  auto dma1 = [&](int t, int stage, int d) {
    if (d < 4) blds16(rx, xoff[d], t * 128, X0_ + stage * XST + (32 * wave + 8 * d) * 128, smem);
    else {
      const int k = d - 4; int wi = wi0 + k; bool up = wi >= 18; int gi = up ? wi - 18 : wi;
      blds16(up ? ru : rg, woff[k], t * 64 * 9216, W0_ + stage * WST + (up ? UOFF : 0) + gi * 1024, smem);
    }
  };
  float gs0 = 0.f, gs1 = 0.f;
  u32 ux0 = 0, ux1 = 0, ugw = 0;
  const int urow = 8 * jb + 2 * (wave - 4);
  const u32 uaddr = X0_ + urow * 128 + (((lane >> 3) ^ ((urow >> 1) & 7)) << 4) + (lane & 7) * 2;
  auto sub = [&](Frag<NT>& cur, Frag<NT>& nxt, bool do_mma, int stage, int s, int t1, int d0, int nd) {
    typedef short s16x8 __attribute__((ext_vector_type(8)));
    s16x4 ta[NT], tb[NT];
    int di = 0;
#pragma unroll
    for (int i = 0; i < 2 * NT; ++i) {
      if (do_mma) acc[i >> 1][i & 1] = __builtin_amdgcn_mfma_f32_32x32x16_bf16(cur.w[i >> 1], cur.x[i & 1], acc[i >> 1][i & 1], 0, 0, 0);
      if (i == 0) {
        nxt.x[0] = *(const LAS bf16x8*)(LAS char*)(smem + xa[s] + stage * XST);
        nxt.x[1] = *(const LAS bf16x8*)(LAS char*)(smem + xa[s] + stage * XST + 4096);
      } else if (i == 1) {
        ta[0] = tr_read(smem, wa + stage * WST + (16 * s) * 288);
        tb[0] = tr_read(smem, wa + stage * WST + (16 * s + 4) * 288);
      } else {
        const int T = i >> 1;
        if ((i & 1) == 0) ta[T] = tr_read(smem, wa + stage * WST + (16 * s) * 288 + 32 * T);
        else              tb[T] = tr_read(smem, wa + stage * WST + (16 * s + 4) * 288 + 32 * T);
      }
      if (WN == 1) {
        if (s == 0 && i == 7) {
          ux0 = *(const LAS u16*)(LAS char*)(smem + uaddr + stage * XST);
          ux1 = *(const LAS u16*)(LAS char*)(smem + uaddr + stage * XST + 128);
          ugw = *(const LAS u16*)(LAS char*)(smem + GWL + ((t1 - 1) * 64 + lane) * 2);
        }
        if (s == 1 && i == 4) gs0 += rbf(__uint_as_float(ux0 << 16) * __uint_as_float(ugw << 16));
        if (s == 1 && i == 5) gs1 += rbf(__uint_as_float(ux1 << 16) * __uint_as_float(ugw << 16));
        if (s == 1 && i == 6) *(LAS u32*)(LAS char*)(smem + UTS + ((t1 - 1) * 64 + lane) * 16 + (wave - 4) * 4) = ux0 | (ux1 << 16);
      }
      if (di < nd && (i % DMA_EVERY) == (WN ? 1 : 2) % DMA_EVERY) { dma1(t1, stage ^ 1, d0 + di); ++di; }
      SCHED0;
    }
#pragma unroll
    for (int k = 0; k < 9; ++k) if (di < nd) { dma1(t1, stage ^ 1, d0 + di); ++di; }
#pragma unroll
    for (int T = 0; T < NT; ++T) nxt.w[T] = __builtin_bit_cast(bf16x8, __builtin_shufflevector(ta[T], tb[T], 0, 1, 2, 3, 4, 5, 6, 7));
    SCHED0;
  };
  auto wrelay = [&]() {
    int lane = lane_; asm volatile("" : "+v"(lane));
    const int g4 = lane >> 4, i16 = lane & 15, hh2 = g4 & 1;
#pragma unroll
    for (int r = 0; r < 3; ++r) {
      const int item = 2 * (wave + 8 * r) + (g4 >> 1);
      if (wave + 8 * r < 23) {
        const int it = item < 45 ? item : 44, T = it / 5, m = it - 5 * T;
        const u32 la = WDN + (16 * T + 4 * hh2 + (i16 >> 2)) * 144 + m * 32 + (i16 & 3) * 8;
        s16x4 a = tr_read(smem, la), b = tr_read(smem, la + 8 * 144);
        typedef short s16x8 __attribute__((ext_vector_type(8)));
        const u32x4 v = __builtin_bit_cast(u32x4, __builtin_shufflevector(a, b, 0, 1, 2, 3, 4, 5, 6, 7));
        const int G = 9 * jb + T, qq = 2 * (G & 1) + hh2;
        char* dst = ws + WS_WF + (size_t)(ib * 144 + (G >> 1)) * 4608 + (m < 4 ? m * 1024 + (qq * 16 + i16) * 16 : 4096 + (qq * 8 + i16) * 16);
        if (item < 45 && (m < 4 || i16 < 8)) *(u32x4*)dst = v;
      }
    }
  };
  u32 pfd = 0;
  u32 pfo;
  if (WN == 0) { const int l2 = (ib & 3) * 48 + wave * 12 + lane, row = l2 / 3, c = l2 - 3 * row; pfo = row * 9216 + (c < 2 ? 256 * jb + 128 * c : 8192 + 32 * jb); }
  else pfo = ((jb & 3) * 64 + (wave - 4) * 16 + lane) * 1152;
  const rsrc_t rpf = WN == 0 ? mk_rsrc(ib >= 4 ? wu : wg, D_ * F_ * 2) : rx;
  auto pf = [&](int tp) {
    if (tp <= 8) {
      const u32 so = WN == 0 ? tp * 64 * 9216 : tp * 128;
      if (lane < (WN == 0 ? 12 : 16)) asm volatile("buffer_load_dword %0, %1, %2, %3 offen" : "+v"(pfd) : "v"(pfo), "s"(rpf), "s"(so) : "memory");
    } else {
      int ln = lane_; asm volatile("" : "+v"(ln));
      const int li = min(wave * 36 + ln, 287), row = li >> 1, T = row >> 4;
      const u32 o2 = ((T < 8 ? 128 * jb + 16 * T : 4096 + 16 * jb) + (row & 15)) * 1152 + ib * 144 + (li & 1) * 128;
      const rsrc_t rd = mk_rsrc(wd, F_ * D_ * 2);
      if (ln < 36) asm volatile("buffer_load_dword %0, %1, %2, 0 offen" : "+v"(pfd) : "v"(min(o2, (u32)(F_ * D_ * 2 - 4))), "s"(rd) : "memory");
    }
  };
  auto wdn_dma = [&]() {
    int lane = lane_; asm volatile("" : "+v"(lane));
    const rsrc_t rd = mk_rsrc((const char*)wd + ib * 144, F_ * D_ * 2 - ib * 144);
#pragma unroll
    for (int r = 0; r < 3; ++r) {
      const int k = wave + 8 * r, ci = 64 * k + lane;
      if (k < 21) {
        if (ci < 1296) {
          const int row = ci / 9, cc = ci - 9 * row, T = row >> 4;
          const int f = (T < 8 ? 128 * jb + 16 * T : 4096 + 16 * jb) + (row & 15);
          blds16(rd, f * 1152 + cc * 16, 0, WDN + k * 1024, smem);
        }
      }
    }
  };
  auto step = [&](int t, int stage, bool first, bool last) {
    {
      int ne = 0;
      ne += first ? 2 : (t - 1 <= 6 ? 1 : 0);
      if (ne == 0) WAITV0;
      else if (ne == 1) asm volatile("s_waitcnt vmcnt(1)" ::: "memory");
      else if (ne == 2) asm volatile("s_waitcnt vmcnt(2)" ::: "memory");
      else if (ne == 3) asm volatile("s_waitcnt vmcnt(3)" ::: "memory");
      else asm volatile("s_waitcnt vmcnt(4)" ::: "memory");
    }
    WAITL0; BAR; SCHED0;
    if (last) { wdn_dma(); SCHED0; }
    constexpr int ND = 4 + NW;
    constexpr int N0 = DMA_N0, N1 = (ND - N0 < DMA_N1) ? ND - N0 : DMA_N1, N2 = ND - N0 - N1;
    sub(f1, f0, !first, stage, 0, t + 1, 0, last ? 0 : N0);
    sub(f0, f1, true, stage, 1, t + 1, N0, last ? 0 : N1);
    sub(f1, f0, true, stage, 2, t + 1, N0 + N1, last ? 0 : N2);
    SCHED0;
    if (t <= 6) pf(t + 3);
    SCHED0;
    sub(f0, f1, true, stage, 3, t + 1, 0, 0);
  };
  if (wave == 4) {
    const rsrc_t rgw = mk_rsrc(gw, D_ * 2);
    blds16(rgw, lane * 16, 0, GWL, smem);
    if (lane < 8) blds16(rgw, 1024 + lane * 16, 0, GWL + 1024, smem);
  }
#pragma unroll
  for (int p = 0; p < 4 + NW; ++p) dma1(0, 0, p);
pf(1); pf(2);
  step(0, 0, true, false);
  for (int t = 1; t < 7; t += 2) { step(t, 1, false, false); step(t + 1, 0, false, false); }
  step(7, 1, false, false);
  step(8, 0, false, true);
  k1_mma<NT>(acc, f1);
  WAITV0; SCHED0;
  asm volatile("" :: "v"(pfd));
  if (WN == 0 && wave == 0) {
    int ln = lane_; asm volatile("" : "+v"(ln));
    const int a = min(ln / 6, 5), c = ln % 6;
    const u16* pa = a == 0 ? p0 : a == 1 ? p1 : a == 2 ? p2 : a == 3 ? p3 : a == 4 ? p4 : p5;
    if (blockIdx.x < 192 && ln < 36) asm volatile("global_load_dword %0, %1, off" : "+v"(pfd) : "v"(pa + blockIdx.x * 384 + c * 64) : "memory");
  }
  if (WN == 1) {
#pragma unroll
    for (int o2 = 32; o2 > 0; o2 >>= 1) { gs0 += __shfl_xor(gs0, o2); gs1 += __shfl_xor(gs1, o2); }
    const float v = rbf(lane == 0 ? gs0 : gs1);
    if (lane < 2) ((float*)(ws + WS_G))[256 * ib + urow + lane] = rbf(1.f / (1.f + expf(-v)));
    if (blockIdx.x == 0 && wave == 4 && lane < 8) *(u32*)(ws + WS_CNT + lane * 256) = 0u;
  }

  char* hb = ws + WS_H + (size_t)(4 * ib + wm) * 144 * 4096 + ((r32 >> 4) * 1024 + (hh * 16 + (r32 & 15)) * 16);
#pragma unroll
  for (int T = 0; T < NT; ++T) {
    const int G = 9 * jb + T + (WN ? 5 : 0);
#pragma unroll
    for (int xi = 0; xi < 2; ++xi) {
      u32x4 o;
      u32 qq[8];
#pragma unroll
      for (int i = 0; i < 8; ++i) qq[i] = pk_bf16(acc[T][xi][i], acc[T][xi][8 + i]);
      mul_halves8(qq);
#pragma unroll
      for (int i = 0; i < 4; ++i) o[i] = pk_bf16(__uint_as_float(qq[2 * i]), __uint_as_float(qq[2 * i + 1]));
      *(u32x4*)(hb + (size_t)(G >> 1) * 4096 + xi * 2048 + (G & 1) * 512) = o;
    }
  }
  WAITL0; BAR; SCHED0;
  wrelay();
  {
    const int tid = wave * 64 + lane;
    char* ut = ws + WS_UT + (256 * ib + 8 * jb) * 2;
    *(u32x4*)(ut + (size_t)tid * 4096) = *(const LAS u32x4*)(LAS char*)(smem + UTS + tid * 16);
    if (tid < 64) *(u32x4*)(ut + (size_t)(512 + tid) * 4096) = *(const LAS u32x4*)(LAS char*)(smem + UTS + (512 + tid) * 16);
  }
}

__global__ __launch_bounds__(512, 2) void k1_gemm1(const u16* __restrict__ x, const u16* __restrict__ wg, const u16* __restrict__ wu,
                                                   const u16* __restrict__ wd, const u16* __restrict__ gw, char* __restrict__ ws,
                                                   const u16* p0, const u16* p1, const u16* p2, const u16* p3, const u16* p4, const u16* p5) {
  extern __shared__ __attribute__((aligned(16))) char smem[];
  const int b = blockIdx.x, idx = b >> 3;
  const int ib = idx & 7, jb = 4 * (b & 7) + (idx >> 3);
  const int wave = __builtin_amdgcn_readfirstlane(threadIdx.x >> 6), lane = threadIdx.x & 63;
  if (wave < 4) k1_body<5, 0>(smem, x, wg, wu, wd, gw, ws, ib, jb, wave, lane, p0, p1, p2, p3, p4, p5);
  else          k1_body<4, 1>(smem, x, wg, wu, wd, gw, ws, ib, jb, wave, lane, p0, p1, p2, p3, p4, p5);
}

#define S2V(i)
#define S2R(i)
#define S2ID
constexpr int K2_LDS = 72672;
constexpr int NCONV = 192;
constexpr u32 AUX_SC1 = 16;

__device__ __forceinline__ u32x4 bload16(rsrc_t r, u32 voff, u32 soff) {
  return __builtin_bit_cast(u32x4, __builtin_amdgcn_raw_buffer_load_b128(r, voff, soff, 0));
}
__device__ __forceinline__ int gbase(int G) {
  int jb = G / 9, T = G - 9 * jb;
  return T < 8 ? 128 * jb + 16 * T : 4096 + 16 * jb;
}

#define G2D 3
__device__ __forceinline__ void gemm2_role(char* smem, char* __restrict__ ws, u16* __restrict__ out,
                                           int ib, int idx, int wave, int lane) {
  const int rowsub = idx & 3, cb = idx >> 2, rb64 = 4 * ib + rowsub;
  const rsrc_t rh = mk_rsrc(ws + WS_H + (size_t)rb64 * 144 * 4096, 144 * 4096);
  const rsrc_t rw = mk_rsrc(ws + WS_WF + (size_t)cb * 144 * 4608, 144 * 4608);
  const int q = lane >> 4, i16 = lane & 15;
  f32x4 acc[5][4];
#pragma unroll
  for (int a = 0; a < 5; ++a)
#pragma unroll
    for (int b = 0; b < 4; ++b) acc[a][b] = (f32x4)(0.f);
  u32x4 hb[G2D][4], wb[G2D][5];
  const u32 hv = lane * 16, hv4 = 4096 + (q * 8 + (i16 & 7)) * 16;
  auto ldh = [&](int j, int r, int ks) { hb[j][r] = bload16(rh, hv + r * 1024, ks * 4096); };
  auto ldw = [&](int j, int m, int ks) { wb[j][m] = m < 4 ? bload16(rw, hv + m * 1024, ks * 4608) : bload16(rw, hv4, ks * 4608); };
  auto ldall = [&](int j, int ks) {
#pragma unroll
    for (int r = 0; r < 3; ++r) ldh(j, r, ks);
#pragma unroll
    for (int m = 0; m < 5; ++m) ldw(j, m, ks);
    ldh(j, 3, ks);
    SCHED0;
  };
  auto kstep = [&](int j, int ksn, bool more) {
#pragma unroll
    for (int r = 0; r < 4; ++r) {
#pragma unroll
      for (int m = 0; m < 5; ++m) {
        acc[m][r] = __builtin_amdgcn_mfma_f32_16x16x32_bf16(__builtin_bit_cast(bf16x8, wb[j][m]), __builtin_bit_cast(bf16x8, hb[j][r]), acc[m][r], 0, 0, 0);
        if (r == 3 && more) { SCHED0; ldw(j, m, ksn); SCHED0; }
      }
      SCHED0;
      if (more) ldh(j, r, ksn);
      SCHED0;
    }
  };
  S2V(0); S2R(14); S2ID;
#pragma unroll
  for (int j = 0; j < G2D; ++j) ldall(j, wave + 4 * j);
  SCHED0;
  constexpr int NIT = 36 / G2D;
  const u32* cnt = (const u32*)(ws + WS_CNT + cb * 256);
  u32 early = 0;
  for (int it = 0; it < NIT - 1; ++it) {
    if (it == NIT - 2) early = __hip_atomic_load(cnt, __ATOMIC_RELAXED, __HIP_MEMORY_SCOPE_AGENT);
#pragma unroll
    for (int j = 0; j < G2D; ++j) kstep(j, wave + 4 * (G2D * (it + 1) + j), true);
    if (it == 0) S2V(1);
  }
  S2V(2);
  const int tid = wave * 64 + lane;
  u32x4 av[3];
  const float gv = ((const float*)(ws + WS_G))[rb64 * 64 + 16 * wave + i16];
  const rsrc_t ra = mk_rsrc(ws + WS_AT + (size_t)cb * 72 * 4096 + rb64 * 128, 72 * 4096);
  auto lda = [&]() {
#pragma unroll
    for (int k = 0; k < 3; ++k) {
      const int ci = min(tid + 256 * k, 575);
      av[k] = __builtin_bit_cast(u32x4, __builtin_amdgcn_raw_buffer_load_b128(ra, (ci >> 3) * 4096 + (ci & 7) * 16, 0, AUX_SC1));
    }
  };
  const bool ready = __builtin_amdgcn_readfirstlane(early) >= NCONV / 8;
  if (ready) lda();
  SCHED0;
  S2V(3);
#pragma unroll
  for (int j = 0; j < G2D; ++j) kstep(j, 0, false);
  S2V(4);

#pragma unroll
  for (int r = 0; r < 4; ++r) {
    if (r != wave) {
      const int slot = (wave - r - 1) & 3;
#pragma unroll
      for (int dt = 0; dt < 5; ++dt)
        *(LAS f32x4*)(LAS char*)(smem + ((r * 3 + slot) * 5 + dt) * 1024 + lane * 16) = acc[dt][r];
    }
  }
  f32x4 own[5];
#pragma unroll
  for (int dt = 0; dt < 5; ++dt) own[dt] = wave == 0 ? acc[dt][0] : wave == 1 ? acc[dt][1] : wave == 2 ? acc[dt][2] : acc[dt][3];
  if (!ready) {
    for (int it = 0; it < (1 << 22); ++it) {
      if (__hip_atomic_load(cnt, __ATOMIC_RELAXED, __HIP_MEMORY_SCOPE_AGENT) >= NCONV / 8) break;
      __builtin_amdgcn_s_sleep(2);
    }
    lda();
  }
#pragma unroll
  for (int k = 0; k < 3; ++k) {
    const int ci = tid + 256 * k;
    if (ci < 576) *(LAS u32x4*)(LAS char*)(smem + 61440 + (ci >> 3) * 144 + (ci & 7) * 16) = av[k];
  }
  S2V(5);
  WAITL0; BAR;
  u16* orow = out + (size_t)(rb64 * 64 + 16 * wave + i16) * D_ + cb * 72 + 4 * q;
  const float omg = rbf(1.f - gv);
#pragma unroll
  for (int dt = 0; dt < 5; ++dt) {
    f32x4 s = own[dt];
#pragma unroll
    for (int sl = 0; sl < 3; ++sl) s += *(const LAS f32x4*)(LAS char*)(smem + ((wave * 3 + sl) * 5 + dt) * 1024 + lane * 16);
    if (dt < 4 || q < 2) {
      float o[4];
#pragma unroll
      for (int i = 0; i < 4; ++i) {
        float av = bf2f(*(const LAS u16*)(LAS char*)(smem + 61440 + (16 * dt + 4 * q + i) * 144 + (16 * wave + i16) * 2));
        o[i] = rbf(gv * av) + rbf(omg * rbf(s[i]));
      }
      u32x2 pv = {pk_bf16(o[0], o[1]), pk_bf16(o[2], o[3])};
      *(u32x2*)(orow + 16 * dt) = pv;
    }
  }
  S2V(6);
  S2V(7); S2R(15);
}

constexpr int RS = 528, TABE = 41 * RS, CPS = 4160, CPY = 4 * CPS, KV = 3 * CPY, KVS = 2976, US = KV + 3 * KVS, USTR = 4608;
struct CP { f32x2 v, r; };
__device__ __forceinline__ CP mkcp(f32x2 p) { CP c; c.v = p; c.r = (f32x2){-p.y, p.x}; return c; }
__device__ __forceinline__ f32x2 cmul(f32x2 a, const CP& p) {
  return __builtin_elementwise_fma(p.r, (f32x2){a.y, a.y}, p.v * (f32x2){a.x, a.x});
}
__device__ __forceinline__ f32x2 cexp_(float ar, float air) {
  float e = __builtin_amdgcn_exp2f(ar * 1.44269504088896341f);
  float fr = __builtin_amdgcn_fractf(air);
  return (f32x2){e * __builtin_amdgcn_cosf(fr), e * __builtin_amdgcn_sinf(fr)};
}
__device__ __forceinline__ float wave_max_nonneg(float m) {
  int v = __float_as_int(m);
  v = max(v, __builtin_amdgcn_update_dpp(0, v, 0x111, 0xf, 0xf, true));
  v = max(v, __builtin_amdgcn_update_dpp(0, v, 0x112, 0xf, 0xf, true));
  v = max(v, __builtin_amdgcn_update_dpp(0, v, 0x114, 0xf, 0xf, true));
  v = max(v, __builtin_amdgcn_update_dpp(0, v, 0x118, 0xf, 0xf, true));
  const int a = __builtin_amdgcn_readlane(v, 15), b = __builtin_amdgcn_readlane(v, 31);
  const int c = __builtin_amdgcn_readlane(v, 47), d = __builtin_amdgcn_readlane(v, 63);
  return __int_as_float(max(max(a, b), max(c, d)));
}
__device__ __forceinline__ void st_h2(char* smem, u32 off, f32x2 v) {
  *(LAS f16x2*)(LAS char*)(smem + off) = __builtin_convertvector(v, f16x2);
}

__device__ __forceinline__ void conv_role(char* smem, const u16* __restrict__ are, const u16* __restrict__ aim, const u16* __restrict__ bre,
                                          const u16* __restrict__ bim, const u16* __restrict__ cre, const u16* __restrict__ cim,
                                          const u16* __restrict__ logdt, const u16* __restrict__ dskip, char* __restrict__ ws,
                                          int cw, int wave, int lane) {
  const int tid = wave * 64 + lane, n = tid & 127, half = wave >> 1;
  const int d0 = 3 * cw;
  S2V(0); S2R(14); S2ID;
  u16 raw[3][10], rdt[3], rds[3];
#pragma unroll
  for (int ch = 0; ch < 3; ++ch) {
    const int o = (d0 + ch) * NS_ + n, o2 = o ^ 64;
    rdt[ch] = logdt[d0 + ch]; rds[ch] = dskip[d0 + ch];
    raw[ch][0] = are[o]; raw[ch][1] = aim[o]; raw[ch][2] = bre[o]; raw[ch][3] = bim[o]; raw[ch][4] = cre[o]; raw[ch][5] = cim[o];
    raw[ch][6] = bre[o2]; raw[ch][7] = bim[o2]; raw[ch][8] = cre[o2]; raw[ch][9] = cim[o2];
  }
#pragma unroll
  for (int ch = 0; ch < 3; ++ch)
    glds16(ws + WS_UT + (size_t)(d0 + ch) * 4096 + tid * 16, US + ch * USTR + 480 + wave * 1024, smem);
  SCHED0;
  if (tid < 90) *(LAS u32x4*)(LAS char*)(smem + US + (tid / 30) * USTR + (tid % 30) * 16) = (u32x4)(0u);
  u32 gidx[11];
  {
    const int J = 2044 - 8 * tid;
#pragma unroll
    for (int x = -7; x <= 3; ++x) {
      int j = J + x;
      u32 bits = pk_bf16((float)j, 0.f) & 0xffffu;
      int idx = j < 256 ? j : (int)bits - 17024;
      gidx[x + 7] = KV + (j < 0 ? 740 : idx + (idx >> 3)) * 4;
    }
  }
  const int q = lane >> 4, i16 = lane & 15;
  u32 erow;
  {
    int s = wave, b = i16;
#pragma unroll
    for (int k = 0; k < 3; ++k) if (s > 0 && b < 8) { --s; b *= 2; }
    erow = TABE + (s == 0 ? b : 8 * s + b) * RS + q * 16;
  }
  SCHED0;
  float par[3][4], dsk[3], wsc[3];
#pragma unroll
  for (int ch = 0; ch < 3; ++ch) {
    const float dt = rbf(expf(bf2f(rdt[ch])));
    dsk[ch] = bf2f(rds[ch]);
    const float ar = dt * bf2f(raw[ch][0]), ai = dt * bf2f(raw[ch][1]) * 0.15915494309189535f;
    float br = dt * bf2f(raw[ch][2]), bi = dt * bf2f(raw[ch][3]), cr = bf2f(raw[ch][4]), ci = bf2f(raw[ch][5]);
    const float wr = __builtin_fmaf(cr, br, -(ci * bi)), wi = __builtin_fmaf(cr, bi, ci * br);
    br = dt * bf2f(raw[ch][6]); bi = dt * bf2f(raw[ch][7]); cr = bf2f(raw[ch][8]); ci = bf2f(raw[ch][9]);
    const float wr2 = __builtin_fmaf(cr, br, -(ci * bi)), wi2 = __builtin_fmaf(cr, bi, ci * br);
    float m = fmaxf(fmaxf(fabsf(wr), fabsf(wi)), fmaxf(fabsf(wr2), fabsf(wi2)));
    m = wave_max_nonneg(m);
    int e = m > 0.f ? __builtin_amdgcn_frexp_expf(m) : 14;
    e = e < -100 ? -100 : e;
    wsc[ch] = __builtin_ldexpf(1.f, e - 14);
    const float sc = __builtin_ldexpf(1.f, 14 - e);
    par[ch][0] = half ? ar * 16.f : ar; par[ch][1] = half ? ai * -16.f : ai;
    par[ch][2] = half ? wr * sc : 1.f; par[ch][3] = half ? -wi * sc : 0.f;
  }
  S2V(1);
  const u32 tabw = (half ? 0 : TABE) + n * 4;
  const u32 xrow = ((wave == 0 ? 0 : 8 + 8 * wave) + i16) * RS + q * 16;
  u32 kvw[4];
  {
    const int nrow = wave == 0 ? 16 : wave == 3 ? 9 : 8, r0 = wave == 0 ? 0 : 8 + 8 * wave;
#pragma unroll
    for (int i = 0; i < 4; ++i) kvw[i] = KV + (4 * q + i < nrow ? (r0 + 4 * q + i) * 18 + i16 + (i16 >> 3) : 741) * 4;
  }
  if (tid < 3) *(LAS float*)(LAS char*)(smem + KV + tid * KVS + 740 * 4) = 0.f;
  for (int ch = -1; ch < 3; ++ch) {
    f32x4 kacc = (f32x4)(0.f), kacc2 = (f32x4)(0.f);
    if (ch >= 0) {
      f16x8 ka[8], kb_[8];
#pragma unroll
      for (int kb = 0; kb < 8; ++kb) {
        ka[kb] = *(const LAS f16x8*)(LAS char*)(smem + xrow + kb * 64);
        kb_[kb] = *(const LAS f16x8*)(LAS char*)(smem + erow + kb * 64);
      }
      WAITL0; BAR;
      if (ch == 0) S2V(3);
#pragma unroll
      for (int kb = 0; kb < 8; kb += 2) {
        kacc = __builtin_amdgcn_mfma_f32_16x16x32_f16(ka[kb], kb_[kb], kacc, 0, 0, 0);
        kacc2 = __builtin_amdgcn_mfma_f32_16x16x32_f16(ka[kb + 1], kb_[kb + 1], kacc2, 0, 0, 0);
      }
      SCHED0;
    }
    if (ch < 2) {
      const int c1 = ch + 1;
      const float ar = c1 == 0 ? par[0][0] : c1 == 1 ? par[1][0] : par[2][0];
      const float ai = c1 == 0 ? par[0][1] : c1 == 1 ? par[1][1] : par[2][1];
      const f32x2 W = {c1 == 0 ? par[0][2] : c1 == 1 ? par[1][2] : par[2][2], c1 == 0 ? par[0][3] : c1 == 1 ? par[1][3] : par[2][3]};
      CP P[8];
      P[0] = mkcp(cexp_(ar, ai));
#pragma unroll
      for (int i = 1; i < 4; ++i) P[i] = mkcp(cmul(P[i - 1].v, P[i - 1]));
      P[4] = mkcp(cexp_(ar * 16.f, ai * 16.f));
#pragma unroll
      for (int i = 5; i < 8; ++i) P[i] = mkcp(cmul(P[i - 1].v, P[i - 1]));
      f32x2 X[16];
      X[0] = W; X[1] = cmul(W, P[0]); X[2] = cmul(W, P[1]); X[3] = cmul(X[1], P[1]);
#pragma unroll
      for (int k = 0; k < 4; ++k) X[4 + k] = cmul(X[k], P[2]);
#pragma unroll
      for (int k = 0; k < 8; ++k) X[8 + k] = cmul(X[k], P[3]);
#pragma unroll
      for (int a = 0; a < 16; ++a) st_h2(smem, tabw + a * RS, X[a]);
#pragma unroll
      for (int s = 1; s < 4; ++s) {
        f32x2 Y[8];
        Y[0] = cmul(W, P[s + 3]); Y[1] = cmul(Y[0], P[s]); Y[2] = cmul(Y[0], P[s + 1]); Y[3] = cmul(Y[1], P[s + 1]);
#pragma unroll
        for (int k = 0; k < 4; ++k) Y[4 + k] = cmul(Y[k], P[s + 2]);
#pragma unroll
        for (int a = 0; a < 8; ++a) st_h2(smem, tabw + (16 + 8 * (s - 1) + a) * RS, Y[a]);
      }
      st_h2(smem, tabw + 40 * RS, cmul(W, P[7]));
    }
    if (ch >= 0) {
      SCHED0;
      const float ksc = ch == 0 ? wsc[0] : ch == 1 ? wsc[1] : wsc[2];
      kacc += kacc2;
#pragma unroll
      for (int i = 0; i < 4; ++i) *(LAS float*)(LAS char*)(smem + kvw[i] + ch * KVS) = kacc[i] * ksc;
    }
    WAITL0; BAR;
    if (ch == -1) S2V(2);
    if (ch == 0) S2V(4);
  }
  S2V(5);
  {
    float V[3][11];
#pragma unroll
    for (int ch = 0; ch < 3; ++ch)
#pragma unroll
      for (int x = 0; x < 11; ++x) V[ch][x] = *(const LAS float*)(LAS char*)(smem + gidx[x] + ch * KVS);
    SCHED0;
#pragma unroll
    for (int ch = 0; ch < 3; ++ch) {
      u32 P[11];
#pragma unroll
      for (int x = 1; x < 11; ++x) P[x] = pk_bf16(V[ch][x], V[ch][x - 1]);
#pragma unroll
      for (int sg = 0; sg < 4; ++sg) {
        u32x4 c = {P[sg + 7], P[sg + 5], P[sg + 3], P[sg + 1]};
        *(LAS u32x4*)(LAS char*)(smem + ch * CPY + sg * CPS + tid * 16) = c;
      }
    }
  }
  if (tid < 24) *(LAS u32x4*)(LAS char*)(smem + (tid >> 1) * CPS + 4096 + (tid & 1) * 16) = (u32x4)(0u);
  WAITV0; WAITL0; BAR;
  S2V(6);
  auto finish = [&](const f32x4 s, int ch, int i) {
    const float dskc = ch == 0 ? dsk[0] : ch == 1 ? dsk[1] : dsk[2];
    const rsrc_t ra = mk_rsrc(ws + WS_AT + (size_t)(d0 + ch) * 4096, 4096);
    const int t = 256 * i + 16 * i16 + 4 * q;
    u32x2 uu = *(const LAS u32x2*)(LAS char*)(smem + US + ch * USTR + 480 + t * 2);
    float a0 = rbf(s[0]) + rbf(lo_f(uu[0]) * dskc), a1 = rbf(s[1]) + rbf(hi_f(uu[0]) * dskc);
    float a2 = rbf(s[2]) + rbf(lo_f(uu[1]) * dskc), a3 = rbf(s[3]) + rbf(hi_f(uu[1]) * dskc);
    u32x2 pv = {pk_bf16(a0, a1), pk_bf16(a2, a3)};
    __builtin_amdgcn_raw_buffer_store_b64(pv, ra, t * 2, 0, AUX_SC1);
  };
  const u32 abase0 = (i16 & 3) * CPS + (2044 - 16 + 8 * q - 4 * (i16 >> 2)) * 2;
  const u32 bbase0 = US + 480 + (16 * i16 - 16 + 8 * q) * 2;
  if (wave < 3) {
    constexpr int NTL = 8;
    f32x4 Y[NTL];
#pragma unroll
    for (int i = 0; i < NTL; ++i) Y[i] = (f32x4)(0.f);
    const u32 abase = abase0 + wave * CPY, bbase = bbase0 + wave * USTR;
    bf16x8 A0[NTL], B0[NTL], A1[NTL], B1[NTL];
    auto mm = [&](const bf16x8 (&A)[NTL], const bf16x8 (&B)[NTL], bf16x8 (&An)[NTL], bf16x8 (&Bn)[NTL], int cn, bool doit, bool more) {
      const u32 aa = abase - cn * 64, bb = bbase - cn * 64;
      u32x2 lo[NTL], hi[NTL];
      int idx = 0;
      WAITL0; SCHED0;
#pragma unroll
      for (int k = 0; k < NTL; ++k)
#pragma unroll
        for (int pp = 0; pp + k < NTL; ++pp) {
          if (doit) Y[pp + k] = __builtin_amdgcn_mfma_f32_16x16x32_bf16(A[pp], B[k], Y[pp + k], 0, 0, 0);
          if (more) {
            if (idx < NTL) Bn[idx] = *(const LAS bf16x8*)(LAS char*)(smem + bb + idx * 512);
            else if (idx < 2 * NTL) lo[idx - NTL] = *(const LAS u32x2*)(LAS char*)(smem + aa - (idx - NTL) * 512);
            else if (idx < 3 * NTL) hi[idx - 2 * NTL] = *(const LAS u32x2*)(LAS char*)(smem + aa - (idx - 2 * NTL) * 512 + 8);
          }
          ++idx;
          SCHED0;
        }
      if (more) {
#pragma unroll
        for (int pp = 0; pp < NTL; ++pp) An[pp] = __builtin_bit_cast(bf16x8, (u32x4){lo[pp][0], lo[pp][1], hi[pp][0], hi[pp][1]});
      }
    };
    mm(A1, B1, A0, B0, 0, false, true);
    for (int c = 0; c < 8; c += 2) {
      mm(A0, B0, A1, B1, c + 1, true, true);
      mm(A1, B1, A0, B0, c + 2, true, c + 2 < 8);
    }
    S2V(7);
#pragma unroll
    for (int i = 0; i < NTL; ++i) finish(Y[i], wave, i);
    S2V(8);
  }
  WAITV0; BAR;
  S2V(10); S2R(15);
  if (tid == 0) __hip_atomic_fetch_add((u32*)(ws + WS_CNT + (d0 / 72) * 256), 1u, __ATOMIC_RELAXED, __HIP_MEMORY_SCOPE_AGENT);
}

__global__ __launch_bounds__(256, 2) void k2_gemm2_conv(const u16* __restrict__ are, const u16* __restrict__ aim,
    const u16* __restrict__ bre, const u16* __restrict__ bim, const u16* __restrict__ cre, const u16* __restrict__ cim,
    const u16* __restrict__ logdt, const u16* __restrict__ dskip, char* __restrict__ ws, u16* __restrict__ out) {
  extern __shared__ __attribute__((aligned(16))) char smem[];
  const int b = blockIdx.x;
  const int wave = __builtin_amdgcn_readfirstlane(threadIdx.x >> 6), lane = threadIdx.x & 63;
  if (b < NCONV) conv_role(smem, are, aim, bre, bim, cre, cim, logdt, dskip, ws, b, wave, lane);
  else { const int g = b - NCONV; gemm2_role(smem, ws, out, g & 7, g >> 3, wave, lane); }
}

extern "C" void kernel_launch(void* const* d_in, const int* in_sizes, int n_in, void* d_out, int out_size, void* d_ws, size_t ws_size, hipStream_t stream) {
  (void)hipFuncSetAttribute((const void*)k1_gemm1, hipFuncAttributeMaxDynamicSharedMemorySize, K1_LDS);
  (void)hipFuncSetAttribute((const void*)k2_gemm2_conv, hipFuncAttributeMaxDynamicSharedMemorySize, K2_LDS);
  k1_gemm1<<<256, 512, K1_LDS, stream>>>((const u16*)d_in[0], (const u16*)d_in[10], (const u16*)d_in[11], (const u16*)d_in[12], (const u16*)d_in[13], (char*)d_ws,
      (const u16*)d_in[1], (const u16*)d_in[2], (const u16*)d_in[3], (const u16*)d_in[4], (const u16*)d_in[5], (const u16*)d_in[6]);
  k2_gemm2_conv<<<NCONV + 256, 256, K2_LDS, stream>>>((const u16*)d_in[1], (const u16*)d_in[2], (const u16*)d_in[3], (const u16*)d_in[4],
      (const u16*)d_in[5], (const u16*)d_in[6], (const u16*)d_in[7], (const u16*)d_in[8], (char*)d_ws, (u16*)d_out);
}
```

```cpp
#include <hip/hip_runtime.h>
typedef unsigned short u16;
typedef unsigned int u32;
typedef short s16x4 __attribute__((ext_vector_type(4)));
typedef __bf16 bf16x8 __attribute__((ext_vector_type(8)));
typedef __bf16 bf16x2 __attribute__((ext_vector_type(2)));
typedef _Float16 f16x2 __attribute__((ext_vector_type(2)));
typedef _Float16 f16x8 __attribute__((ext_vector_type(8)));
typedef float f32x2 __attribute__((ext_vector_type(2)));
typedef float f32x4 __attribute__((ext_vector_type(4)));
typedef float f32x16 __attribute__((ext_vector_type(16)));
typedef u32 u32x2 __attribute__((ext_vector_type(2)));
typedef u32 u32x4 __attribute__((ext_vector_type(4)));
#define LAS __attribute__((address_space(3)))
#define GAS __attribute__((address_space(1)))

constexpr int L_ = 2048, D_ = 576, NS_ = 128, F_ = 4608;
constexpr size_t WS_H   = 0;
constexpr size_t WS_WF  = WS_H + (size_t)32 * 144 * 4096;
constexpr size_t WS_UT  = WS_WF + (size_t)8 * 144 * 4608;
constexpr size_t WS_AT  = WS_UT + (size_t)D_ * L_ * 2;
constexpr size_t WS_G   = WS_AT + (size_t)D_ * L_ * 2;
constexpr size_t WS_CNT = WS_G + (size_t)L_ * 4;
constexpr size_t WS_DBG = 48u << 20;

__device__ __forceinline__ float bf2f(u16 v) { return __uint_as_float(((u32)v) << 16); }
__device__ __forceinline__ u32 pk_bf16(float lo, float hi) {
  f32x2 v = {lo, hi}; bf16x2 b = __builtin_convertvector(v, bf16x2); return __builtin_bit_cast(u32, b);
}
__device__ __forceinline__ float lo_f(u32 p) { return __uint_as_float(p << 16); }
__device__ __forceinline__ float hi_f(u32 p) { return __uint_as_float(p & 0xffff0000u); }
__device__ __forceinline__ void mul_halves8(u32 (&q)[8]) {
  asm("v_dot2_f32_bf16 %0, %0, %8, 0\n\tv_dot2_f32_bf16 %1, %1, %9, 0\n\tv_dot2_f32_bf16 %2, %2, %10, 0\n\tv_dot2_f32_bf16 %3, %3, %11, 0\n\t"
      "v_dot2_f32_bf16 %4, %4, %12, 0\n\tv_dot2_f32_bf16 %5, %5, %13, 0\n\tv_dot2_f32_bf16 %6, %6, %14, 0\n\tv_dot2_f32_bf16 %7, %7, %15, 0\n\ts_nop 2"
      : "+v"(q[0]), "+v"(q[1]), "+v"(q[2]), "+v"(q[3]), "+v"(q[4]), "+v"(q[5]), "+v"(q[6]), "+v"(q[7])
      : "v"(q[0] >> 16), "v"(q[1] >> 16), "v"(q[2] >> 16), "v"(q[3] >> 16), "v"(q[4] >> 16), "v"(q[5] >> 16), "v"(q[6] >> 16), "v"(q[7] >> 16));
}
__device__ __forceinline__ float rbf(float x) { return lo_f(pk_bf16(x, 0.f)); }

__device__ __forceinline__ void glds16(const void* g, u32 lds_byte, char* smem) {
  __builtin_amdgcn_global_load_lds((const GAS void*)g, (LAS void*)(smem + lds_byte), 16, 0, 0);
}
typedef __amdgpu_buffer_rsrc_t rsrc_t;
__device__ __forceinline__ rsrc_t mk_rsrc(const void* p, u32 bytes) {
  return __builtin_amdgcn_make_buffer_rsrc((void*)p, 0, bytes, 0x00020000);
}
__device__ __forceinline__ void blds16(rsrc_t r, u32 voff, u32 soff, u32 lds_byte, char* smem) {
  __builtin_amdgcn_raw_ptr_buffer_load_lds(r, (LAS void*)(smem + lds_byte), 16, voff, soff, 0, 0);
}
__device__ __forceinline__ s16x4 tr_read(char* smem, u32 off) {
  return __builtin_amdgcn_ds_read_tr16_b64_v4i16((LAS s16x4*)(smem + off));
}
#define WAITV0 asm volatile("s_waitcnt vmcnt(0)" ::: "memory")
#define WAITL0 asm volatile("s_waitcnt lgkmcnt(0)" ::: "memory")
#define BAR __builtin_amdgcn_s_barrier()
#define SCHED0 __builtin_amdgcn_sched_barrier(0)

#define DMA_EVERY 2
#define DMA_N0 4
#define DMA_N1 4
constexpr int X0_ = 0, XST = 32768, W0_ = 65536, WST = 36992, UOFF = 18432 + 128;
constexpr int WDN = X0_ + XST;
constexpr int UTS = W0_ + 2 * WST;
constexpr int GWL = UTS + 9216;
constexpr int K1_LDS = GWL + 1152;

template <int NT>
struct Frag { bf16x8 x[2]; bf16x8 w[NT]; };

template <int NT>
__device__ __forceinline__ void k1_load_frags(Frag<NT>& f, char* smem, u32 xaddr, u32 waddr, int stage, int s) {
#pragma unroll
  for (int xi = 0; xi < 2; ++xi)
    f.x[xi] = *(const LAS bf16x8*)(LAS char*)(smem + xaddr + stage * XST + xi * 4096);
#pragma unroll
  for (int T = 0; T < NT; ++T) {
    s16x4 a = tr_read(smem, waddr + stage * WST + (16 * s) * 288 + 32 * T);
    s16x4 b = tr_read(smem, waddr + stage * WST + (16 * s + 4) * 288 + 32 * T);
    typedef short s16x8 __attribute__((ext_vector_type(8)));
    s16x8 c = __builtin_shufflevector(a, b, 0, 1, 2, 3, 4, 5, 6, 7);
    f.w[T] = __builtin_bit_cast(bf16x8, c);
  }
}
template <int NT>
__device__ __forceinline__ void k1_mma(f32x16 (&acc)[NT][2], const Frag<NT>& f) {
#pragma unroll
  for (int T = 0; T < NT; ++T)
#pragma unroll
    for (int xi = 0; xi < 2; ++xi)
      acc[T][xi] = __builtin_amdgcn_mfma_f32_32x32x16_bf16(f.w[T], f.x[xi], acc[T][xi], 0, 0, 0);
}
template <int NT, int NDMA>
__device__ __forceinline__ void k1_interleave() {
#pragma unroll
  for (int i = 0; i < NT * 2; ++i) {
    __builtin_amdgcn_sched_group_barrier(0x8, 1, 0);
    if (i < 2) __builtin_amdgcn_sched_group_barrier(0x100, 2, 0); else __builtin_amdgcn_sched_group_barrier(0x100, 1, 0);
    if (i < NDMA) __builtin_amdgcn_sched_group_barrier(0x10, 1, 0);
  }
}

template <int NT, int WN>
__device__ __forceinline__ void k1_body(char* smem, const u16* __restrict__ x, const u16* __restrict__ wg, const u16* __restrict__ wu,
                                        const u16* __restrict__ wd, const u16* __restrict__ gw, char* __restrict__ ws, int ib, int jb, int wave, int lane,
                                        const u16* p0, const u16* p1, const u16* p2, const u16* p3, const u16* p4, const u16* p5) {
  constexpr int NW = (WN == 0) ? 4 : 5;
  const int wm = wave & 3;
  u32 xoff[4], woff[NW];
#pragma unroll
  for (int ii = 0; ii < 4; ++ii) {
    int rr = lane >> 3, row = 32 * wave + 8 * ii + rr;
    int c = (lane & 7) ^ ((row >> 1) & 7);
    xoff[ii] = row * 1152 + c * 16;
  }
  const int wi0 = (WN == 0) ? 4 * wave : 16 + 5 * (wave - 4);
#pragma unroll
  for (int k = 0; k < NW; ++k) {
    int wi = wi0 + k; int gi = wi >= 18 ? wi - 18 : wi;
    int ci = 64 * gi + lane, row = ci / 18, w18 = ci - row * 18;
    int colb = (w18 < 16) ? (256 * jb + 16 * w18) : (8192 + 32 * jb + 16 * (w18 - 16));
    woff[k] = row * 9216 + colb;
  }
  const rsrc_t rx = mk_rsrc((const char*)x + (size_t)ib * 256 * 1152, 256 * 1152);
  const rsrc_t rg = mk_rsrc(wg, D_ * F_ * 2), ru = mk_rsrc(wu, D_ * F_ * 2);
  const int r32 = lane & 31, hh = lane >> 5;
  u32 xa[4];
#pragma unroll
  for (int s = 0; s < 4; ++s) xa[s] = X0_ + (64 * wm + r32) * 128 + (((2 * s + hh) ^ ((r32 >> 1) & 7)) * 16);
  const int g4 = lane >> 4;
  const u32 wa = W0_ + ((g4 & 1) ? UOFF : 0) + (8 * (g4 >> 1) + ((lane & 15) >> 2)) * 288 + (lane & 3) * 8 + (WN ? 5 * 32 : 0);

  f32x16 acc[NT][2];
#pragma unroll
  for (int T = 0; T < NT; ++T) { acc[T][0] = (f32x16)(0.f); acc[T][1] = (f32x16)(0.f); }
  Frag<NT> f0, f1;
  const int lane_ = lane;

  auto dma1 = [&](int t, int stage, int d) {
    if (d < 4) blds16(rx, xoff[d], t * 128, X0_ + stage * XST + (32 * wave + 8 * d) * 128, smem);
    else {
      const int k = d - 4; int wi = wi0 + k; bool up = wi >= 18; int gi = up ? wi - 18 : wi;
      blds16(up ? ru : rg, woff[k], t * 64 * 9216, W0_ + stage * WST + (up ? UOFF : 0) + gi * 1024, smem);
    }
  };
  float gs0 = 0.f, gs1 = 0.f;
  u32 ux0 = 0, ux1 = 0, ugw = 0;
  const int urow = 8 * jb + 2 * (wave - 4);
  const u32 uaddr = X0_ + urow * 128 + (((lane >> 3) ^ ((urow >> 1) & 7)) << 4) + (lane & 7) * 2;
  auto sub = [&](Frag<NT>& cur, Frag<NT>& nxt, bool do_mma, int stage, int s, int t1, int d0, int nd) {
    typedef short s16x8 __attribute__((ext_vector_type(8)));
    s16x4 ta[NT], tb[NT];
    int di = 0;
#pragma unroll
    for (int i = 0; i < 2 * NT; ++i) {
      if (do_mma) acc[i >> 1][i & 1] = __builtin_amdgcn_mfma_f32_32x32x16_bf16(cur.w[i >> 1], cur.x[i & 1], acc[i >> 1][i & 1], 0, 0, 0);
      if (i == 0) {
        nxt.x[0] = *(const LAS bf16x8*)(LAS char*)(smem + xa[s] + stage * XST);
        nxt.x[1] = *(const LAS bf16x8*)(LAS char*)(smem + xa[s] + stage * XST + 4096);
      } else if (i == 1) {
        ta[0] = tr_read(smem, wa + stage * WST + (16 * s) * 288);
        tb[0] = tr_read(smem, wa + stage * WST + (16 * s + 4) * 288);
      } else {
        const int T = i >> 1;
        if ((i & 1) == 0) ta[T] = tr_read(smem, wa + stage * WST + (16 * s) * 288 + 32 * T);
        else              tb[T] = tr_read(smem, wa + stage * WST + (16 * s + 4) * 288 + 32 * T);
      }
      if (WN == 1) {
        if (s == 0 && i == 7) {
          ux0 = *(const LAS u16*)(LAS char*)(smem + uaddr + stage * XST);
          ux1 = *(const LAS u16*)(LAS char*)(smem + uaddr + stage * XST + 128);
          ugw = *(const LAS u16*)(LAS char*)(smem + GWL + ((t1 - 1) * 64 + lane) * 2);
        }
        if (s == 1 && i == 4) gs0 += rbf(__uint_as_float(ux0 << 16) * __uint_as_float(ugw << 16));
        if (s == 1 && i == 5) gs1 += rbf(__uint_as_float(ux1 << 16) * __uint_as_float(ugw << 16));
        if (s == 1 && i == 6) *(LAS u32*)(LAS char*)(smem + UTS + ((t1 - 1) * 64 + lane) * 16 + (wave - 4) * 4) = ux0 | (ux1 << 16);
      }
      if (di < nd && (i % DMA_EVERY) == (WN ? 1 : 2) % DMA_EVERY) { dma1(t1, stage ^ 1, d0 + di); ++di; }
      SCHED0;
    }
#pragma unroll
    for (int k = 0; k < 9; ++k) if (di < nd) { dma1(t1, stage ^ 1, d0 + di); ++di; }
#pragma unroll
    for (int T = 0; T < NT; ++T) nxt.w[T] = __builtin_bit_cast(bf16x8, __builtin_shufflevector(ta[T], tb[T], 0, 1, 2, 3, 4, 5, 6, 7));
    SCHED0;
  };
  auto wrelay = [&]() {
    int lane = lane_; asm volatile("" : "+v"(lane));
    const int g4 = lane >> 4, i16 = lane & 15, hh2 = g4 & 1;
#pragma unroll
    for (int r = 0; r < 3; ++r) {
      const int item = 2 * (wave + 8 * r) + (g4 >> 1);
      if (wave + 8 * r < 23) {
        const int it = item < 45 ? item : 44, T = it / 5, m = it - 5 * T;
        const u32 la = WDN + (16 * T + 4 * hh2 + (i16 >> 2)) * 144 + m * 32 + (i16 & 3) * 8;
        s16x4 a = tr_read(smem, la), b = tr_read(smem, la + 8 * 144);
        typedef short s16x8 __attribute__((ext_vector_type(8)));
        const u32x4 v = __builtin_bit_cast(u32x4, __builtin_shufflevector(a, b, 0, 1, 2, 3, 4, 5, 6, 7));
        const int G = 9 * jb + T, qq = 2 * (G & 1) + hh2;
        char* dst = ws + WS_WF + (size_t)(ib * 144 + (G >> 1)) * 4608 + (m < 4 ? m * 1024 + (qq * 16 + i16) * 16 : 4096 + (qq * 8 + i16) * 16);
        if (item < 45 && (m < 4 || i16 < 8)) *(u32x4*)dst = v;
      }
    }
  };
  u32 pfd = 0;
  u32 pfo;
  if (WN == 0) { const int l2 = (ib & 3) * 48 + wave * 12 + lane, row = l2 / 3, c = l2 - 3 * row; pfo = row * 9216 + (c < 2 ? 256 * jb + 128 * c : 8192 + 32 * jb); }
  else pfo = ((jb & 3) * 64 + (wave - 4) * 16 + lane) * 1152;
  const rsrc_t rpf = WN == 0 ? mk_rsrc(ib >= 4 ? wu : wg, D_ * F_ * 2) : rx;
  auto pf = [&](int tp) {
    if (tp <= 8) {
      const u32 so = WN == 0 ? tp * 64 * 9216 : tp * 128;
      if (lane < (WN == 0 ? 12 : 16)) asm volatile("buffer_load_dword %0, %1, %2, %3 offen" : "+v"(pfd) : "v"(pfo), "s"(rpf), "s"(so) : "memory");
    } else {
      int ln = lane_; asm volatile("" : "+v"(ln));
      const int li = min(wave * 36 + ln, 287), row = li >> 1, T = row >> 4;
      const u32 o2 = ((T < 8 ? 128 * jb + 16 * T : 4096 + 16 * jb) + (row & 15)) * 1152 + ib * 144 + (li & 1) * 128;
      const rsrc_t rd = mk_rsrc(wd, F_ * D_ * 2);
      if (ln < 36) asm volatile("buffer_load_dword %0, %1, %2, 0 offen" : "+v"(pfd) : "v"(min(o2, (u32)(F_ * D_ * 2 - 4))), "s"(rd) : "memory");
    }
  };
  auto wdn_dma = [&]() {
    int lane = lane_; asm volatile("" : "+v"(lane));
    const rsrc_t rd = mk_rsrc((const char*)wd + ib * 144, F_ * D_ * 2 - ib * 144);
#pragma unroll
    for (int r = 0; r < 3; ++r) {
      const int k = wave + 8 * r, ci = 64 * k + lane;
      if (k < 21) {
        if (ci < 1296) {
          const int row = ci / 9, cc = ci - 9 * row, T = row >> 4;
          const int f = (T < 8 ? 128 * jb + 16 * T : 4096 + 16 * jb) + (row & 15);
          blds16(rd, f * 1152 + cc * 16, 0, WDN + k * 1024, smem);
        }
      }
    }
  };
  auto step = [&](int t, int stage, bool first, bool last) {
    {
      int ne = 0;
      ne += first ? 2 : (t - 1 <= 6 ? 1 : 0);
      if (ne == 0) WAITV0;
      else if (ne == 1) asm volatile("s_waitcnt vmcnt(1)" ::: "memory");
      else if (ne == 2) asm volatile("s_waitcnt vmcnt(2)" ::: "memory");
      else if (ne == 3) asm volatile("s_waitcnt vmcnt(3)" ::: "memory");
      else asm volatile("s_waitcnt vmcnt(4)" ::: "memory");
    }
    WAITL0; BAR; SCHED0;
    if (last) { wdn_dma(); SCHED0; }
    constexpr int ND = 4 + NW;
    constexpr int N0 = DMA_N0, N1 = (ND - N0 < DMA_N1) ? ND - N0 : DMA_N1, N2 = ND - N0 - N1;
    sub(f1, f0, !first, stage, 0, t + 1, 0, last ? 0 : N0);
    sub(f0, f1, true, stage, 1, t + 1, N0, last ? 0 : N1);
    sub(f1, f0, true, stage, 2, t + 1, N0 + N1, last ? 0 : N2);
    SCHED0;
    if (t <= 6) pf(t + 3);
    SCHED0;
    sub(f0, f1, true, stage, 3, t + 1, 0, 0);
  };
  if (wave == 4) {
    const rsrc_t rgw = mk_rsrc(gw, D_ * 2);
    blds16(rgw, lane * 16, 0, GWL, smem);
    if (lane < 8) blds16(rgw, 1024 + lane * 16, 0, GWL + 1024, smem);
  }
#pragma unroll
  for (int p = 0; p < 4 + NW; ++p) dma1(0, 0, p);
pf(1); pf(2);
  step(0, 0, true, false);
  for (int t = 1; t < 7; t += 2) { step(t, 1, false, false); step(t + 1, 0, false, false); }
  step(7, 1, false, false);
  step(8, 0, false, true);
  k1_mma<NT>(acc, f1);
  WAITV0; SCHED0;
  asm volatile("" :: "v"(pfd));
  if (WN == 0 && wave == 0) {
    int ln = lane_; asm volatile("" : "+v"(ln));
    const int a = min(ln / 6, 5), c = ln % 6;
    const u16* pa = a == 0 ? p0 : a == 1 ? p1 : a == 2 ? p2 : a == 3 ? p3 : a == 4 ? p4 : p5;
    if (blockIdx.x < 192 && ln < 36) asm volatile("global_load_dword %0, %1, off" : "+v"(pfd) : "v"(pa + blockIdx.x * 384 + c * 64) : "memory");
  }
  if (WN == 1) {
#pragma unroll
    for (int o2 = 32; o2 > 0; o2 >>= 1) { gs0 += __shfl_xor(gs0, o2); gs1 += __shfl_xor(gs1, o2); }
    const float v = rbf(lane == 0 ? gs0 : gs1);
    if (lane < 2) ((float*)(ws + WS_G))[256 * ib + urow + lane] = rbf(1.f / (1.f + expf(-v)));
    if (blockIdx.x == 0 && wave == 4 && lane < 32) *(u32*)(ws + WS_CNT + lane * 256) = 0u;
  }

  char* hb = ws + WS_H + (size_t)(4 * ib + wm) * 144 * 4096 + ((r32 >> 4) * 1024 + (hh * 16 + (r32 & 15)) * 16);
#pragma unroll
  for (int T = 0; T < NT; ++T) {
    const int G = 9 * jb + T + (WN ? 5 : 0);
#pragma unroll
    for (int xi = 0; xi < 2; ++xi) {
      u32x4 o;
      u32 qq[8];
#pragma unroll
      for (int i = 0; i < 8; ++i) qq[i] = pk_bf16(acc[T][xi][i], acc[T][xi][8 + i]);
      mul_halves8(qq);
#pragma unroll
      for (int i = 0; i < 4; ++i) o[i] = pk_bf16(__uint_as_float(qq[2 * i]), __uint_as_float(qq[2 * i + 1]));
      *(u32x4*)(hb + (size_t)(G >> 1) * 4096 + xi * 2048 + (G & 1) * 512) = o;
    }
  }
  WAITL0; BAR; SCHED0;
  wrelay();
  {
    const int tid = wave * 64 + lane;
    char* ut = ws + WS_UT + (256 * ib + 8 * jb) * 2;
    *(u32x4*)(ut + (size_t)tid * 4096) = *(const LAS u32x4*)(LAS char*)(smem + UTS + tid * 16);
    if (tid < 64) *(u32x4*)(ut + (size_t)(512 + tid) * 4096) = *(const LAS u32x4*)(LAS char*)(smem + UTS + (512 + tid) * 16);
  }
}

__global__ __launch_bounds__(512, 2) void k1_gemm1(const u16* __restrict__ x, const u16* __restrict__ wg, const u16* __restrict__ wu,
                                                   const u16* __restrict__ wd, const u16* __restrict__ gw, char* __restrict__ ws,
                                                   const u16* p0, const u16* p1, const u16* p2, const u16* p3, const u16* p4, const u16* p5) {
  extern __shared__ __attribute__((aligned(16))) char smem[];
  const int b = blockIdx.x, idx = b >> 3;
  const int ib = idx & 7, jb = 4 * (b & 7) + (idx >> 3);
  const int wave = __builtin_amdgcn_readfirstlane(threadIdx.x >> 6), lane = threadIdx.x & 63;
  if (wave < 4) k1_body<5, 0>(smem, x, wg, wu, wd, gw, ws, ib, jb, wave, lane, p0, p1, p2, p3, p4, p5);
  else          k1_body<4, 1>(smem, x, wg, wu, wd, gw, ws, ib, jb, wave, lane, p0, p1, p2, p3, p4, p5);
}

#define S2V(i)
#define S2R(i)
#define S2ID
constexpr int K2_LDS = 72672;
constexpr int NCONV = 192;
constexpr u32 AUX_SC1 = 16;

__device__ __forceinline__ u32x4 bload16(rsrc_t r, u32 voff, u32 soff) {
  return __builtin_bit_cast(u32x4, __builtin_amdgcn_raw_buffer_load_b128(r, voff, soff, 0));
}
__device__ __forceinline__ int gbase(int G) {
  int jb = G / 9, T = G - 9 * jb;
  return T < 8 ? 128 * jb + 16 * T : 4096 + 16 * jb;
}

#define G2D 2
__device__ __forceinline__ void gemm2_role(char* smem, char* __restrict__ ws, u16* __restrict__ out,
                                           int ib, int idx, int wave, int lane) {
  const int rowsub = idx & 3, cb = idx >> 2, rb64 = 4 * ib + rowsub;
  const rsrc_t rh = mk_rsrc(ws + WS_H + (size_t)rb64 * 144 * 4096, 144 * 4096);
  const rsrc_t rw = mk_rsrc(ws + WS_WF + (size_t)cb * 144 * 4608, 144 * 4608);
  const int q = lane >> 4, i16 = lane & 15;
  f32x4 acc[5][4];
#pragma unroll
  for (int a = 0; a < 5; ++a)
#pragma unroll
    for (int b = 0; b < 4; ++b) acc[a][b] = (f32x4)(0.f);
  u32x4 hb[G2D][4], wb[G2D][5];
  const u32 hv = lane * 16, hv4 = 4096 + (q * 8 + (i16 & 7)) * 16;
  auto ldh = [&](int j, int r, int ks) { hb[j][r] = bload16(rh, hv + r * 1024, ks * 4096); };
  auto ldw = [&](int j, int m, int ks) { wb[j][m] = m < 4 ? bload16(rw, hv + m * 1024, ks * 4608) : bload16(rw, hv4, ks * 4608); };
  auto ldall = [&](int j, int ks) {
#pragma unroll
    for (int r = 0; r < 3; ++r) ldh(j, r, ks);
#pragma unroll
    for (int m = 0; m < 5; ++m) ldw(j, m, ks);
    ldh(j, 3, ks);
    SCHED0;
  };
  auto kstep = [&](int j, int ksn, bool more) {
#pragma unroll
    for (int r = 0; r < 4; ++r) {
#pragma unroll
      for (int m = 0; m < 5; ++m) {
        acc[m][r] = __builtin_amdgcn_mfma_f32_16x16x32_bf16(__builtin_bit_cast(bf16x8, wb[j][m]), __builtin_bit_cast(bf16x8, hb[j][r]), acc[m][r], 0, 0, 0);
        if (r == 3 && more) { SCHED0; ldw(j, m, ksn); SCHED0; }
      }
      SCHED0;
      if (more) ldh(j, r, ksn);
      SCHED0;
    }
  };
  S2V(0); S2R(14); S2ID;
#pragma unroll
  for (int j = 0; j < G2D; ++j) ldall(j, wave + 4 * j);
  SCHED0;
  constexpr int NIT = 36 / G2D;
  const u32* cnt = (const u32*)(ws + WS_CNT + (cb * 4 + rowsub) * 256);
  u32 early = 0;
  for (int it = 0; it < NIT - 1; ++it) {
    if (it == NIT - 2) early = __hip_atomic_load(cnt, __ATOMIC_RELAXED, __HIP_MEMORY_SCOPE_AGENT);
#pragma unroll
    for (int j = 0; j < G2D; ++j) kstep(j, wave + 4 * (G2D * (it + 1) + j), true);
    if (it == 0) S2V(1);
  }
  S2V(2);
  const int tid = wave * 64 + lane;
  u32x4 av[3];
  const float gv = ((const float*)(ws + WS_G))[rb64 * 64 + 16 * wave + i16];
  const rsrc_t ra = mk_rsrc(ws + WS_AT + (size_t)cb * 72 * 4096 + rb64 * 128, 72 * 4096);
  auto lda = [&]() {
#pragma unroll
    for (int k = 0; k < 3; ++k) {
      const int ci = min(tid + 256 * k, 575);
      av[k] = __builtin_bit_cast(u32x4, __builtin_amdgcn_raw_buffer_load_b128(ra, (ci >> 3) * 4096 + (ci & 7) * 16, 0, AUX_SC1));
    }
  };
  const bool ready = __builtin_amdgcn_readfirstlane(early) >= NCONV / 8;
  if (ready) lda();
  SCHED0;
  S2V(3);
#pragma unroll
  for (int j = 0; j < G2D; ++j) kstep(j, 0, false);
  S2V(4);

#pragma unroll
  for (int r = 0; r < 4; ++r) {
    if (r != wave) {
      const int slot = (wave - r - 1) & 3;
#pragma unroll
      for (int dt = 0; dt < 5; ++dt)
        *(LAS f32x4*)(LAS char*)(smem + ((r * 3 + slot) * 5 + dt) * 1024 + lane * 16) = acc[dt][r];
    }
  }
  f32x4 own[5];
#pragma unroll
  for (int dt = 0; dt < 5; ++dt) own[dt] = wave == 0 ? acc[dt][0] : wave == 1 ? acc[dt][1] : wave == 2 ? acc[dt][2] : acc[dt][3];
  if (lane == 0) *(LAS u32*)(LAS char*)(smem + 71808 + wave * 4) = ready ? 1u : 0u;
  WAITL0; BAR; asm volatile("" ::: "memory");
  const u32x4 rdy4 = *(const LAS u32x4*)(LAS char*)(smem + 71808);
  const bool allready = __builtin_amdgcn_readfirstlane(rdy4[0] & rdy4[1] & rdy4[2] & rdy4[3]) != 0;
  const float omg = rbf(1.f - gv);
#pragma unroll
  for (int dt = 0; dt < 5; ++dt) {
#pragma unroll
    for (int sl = 0; sl < 3; ++sl) own[dt] += *(const LAS f32x4*)(LAS char*)(smem + ((wave * 3 + sl) * 5 + dt) * 1024 + lane * 16);
#pragma unroll
    for (int i = 0; i < 4; ++i) own[dt][i] = rbf(omg * rbf(own[dt][i]));
  }
  S2V(5);
  if (!allready) {
    if (wave == 0) {
      for (int it = 0; it < (1 << 22); ++it) {
        if (__hip_atomic_load(cnt, __ATOMIC_RELAXED, __HIP_MEMORY_SCOPE_AGENT) >= NCONV / 8) break;
        __builtin_amdgcn_s_sleep(1);
      }
    }
    BAR; asm volatile("" ::: "memory");
    if (!ready) lda();
  }
#pragma unroll
  for (int k = 0; k < 3; ++k) {
    const int ci = tid + 256 * k;
    if (ci < 576) *(LAS u32x4*)(LAS char*)(smem + 61440 + (ci >> 3) * 144 + (ci & 7) * 16) = av[k];
  }
  WAITL0; BAR; asm volatile("" ::: "memory");
  S2V(8);
  u16* orow = out + (size_t)(rb64 * 64 + 16 * wave + i16) * D_ + cb * 72 + 4 * q;
#pragma unroll
  for (int dt = 0; dt < 5; ++dt) {
    if (dt < 4 || q < 2) {
      float o[4];
#pragma unroll
      for (int i = 0; i < 4; ++i) {
        float av = bf2f(*(const LAS u16*)(LAS char*)(smem + 61440 + (16 * dt + 4 * q + i) * 144 + (16 * wave + i16) * 2));
        o[i] = rbf(gv * av) + own[dt][i];
      }
      u32x2 pv = {pk_bf16(o[0], o[1]), pk_bf16(o[2], o[3])};
      *(u32x2*)(orow + 16 * dt) = pv;
    }
  }
  S2V(6);
  S2V(7); S2R(15);
}

constexpr int RS = 528, TABE = 41 * RS, CPS = 4160, CPY = 4 * CPS, KV = 3 * CPY, KVS = 2976, US = KV + 3 * KVS, USTR = 4608;
struct CP { f32x2 v, r; };
__device__ __forceinline__ CP mkcp(f32x2 p) { CP c; c.v = p; c.r = (f32x2){-p.y, p.x}; return c; }
__device__ __forceinline__ f32x2 cmul(f32x2 a, const CP& p) {
  return __builtin_elementwise_fma(p.r, (f32x2){a.y, a.y}, p.v * (f32x2){a.x, a.x});
}
__device__ __forceinline__ f32x2 cexp_(float ar, float air) {
  float e = __builtin_amdgcn_exp2f(ar * 1.44269504088896341f);
  float fr = __builtin_amdgcn_fractf(air);
  return (f32x2){e * __builtin_amdgcn_cosf(fr), e * __builtin_amdgcn_sinf(fr)};
}
__device__ __forceinline__ float wave_max_nonneg(float m) {
  int v = __float_as_int(m);
  v = max(v, __builtin_amdgcn_update_dpp(0, v, 0x111, 0xf, 0xf, true));
  v = max(v, __builtin_amdgcn_update_dpp(0, v, 0x112, 0xf, 0xf, true));
  v = max(v, __builtin_amdgcn_update_dpp(0, v, 0x114, 0xf, 0xf, true));
  v = max(v, __builtin_amdgcn_update_dpp(0, v, 0x118, 0xf, 0xf, true));
  const int a = __builtin_amdgcn_readlane(v, 15), b = __builtin_amdgcn_readlane(v, 31);
  const int c = __builtin_amdgcn_readlane(v, 47), d = __builtin_amdgcn_readlane(v, 63);
  return __int_as_float(max(max(a, b), max(c, d)));
}
__device__ __forceinline__ void st_h2(char* smem, u32 off, f32x2 v) {
  *(LAS f16x2*)(LAS char*)(smem + off) = __builtin_convertvector(v, f16x2);
}

__device__ __forceinline__ void conv_role(char* smem, const u16* __restrict__ are, const u16* __restrict__ aim, const u16* __restrict__ bre,
                                          const u16* __restrict__ bim, const u16* __restrict__ cre, const u16* __restrict__ cim,
                                          const u16* __restrict__ logdt, const u16* __restrict__ dskip, char* __restrict__ ws,
                                          int cw, int wave, int lane) {
  const int tid = wave * 64 + lane, n = tid & 127, half = wave >> 1;
  const int d0 = 3 * cw;
  S2V(0); S2R(14); S2ID;
  u16 raw[3][10], rdt[3], rds[3];
#pragma unroll
  for (int ch = 0; ch < 3; ++ch) {
    const int o = (d0 + ch) * NS_ + n, o2 = o ^ 64;
    rdt[ch] = logdt[d0 + ch]; rds[ch] = dskip[d0 + ch];
    raw[ch][0] = are[o]; raw[ch][1] = aim[o]; raw[ch][2] = bre[o]; raw[ch][3] = bim[o]; raw[ch][4] = cre[o]; raw[ch][5] = cim[o];
    raw[ch][6] = bre[o2]; raw[ch][7] = bim[o2]; raw[ch][8] = cre[o2]; raw[ch][9] = cim[o2];
  }
#pragma unroll
  for (int ch = 0; ch < 3; ++ch)
    glds16(ws + WS_UT + (size_t)(d0 + ch) * 4096 + tid * 16, US + ch * USTR + 480 + wave * 1024, smem);
  SCHED0;
  if (tid < 90) *(LAS u32x4*)(LAS char*)(smem + US + (tid / 30) * USTR + (tid % 30) * 16) = (u32x4)(0u);
  u32 gidx[11];
  {
    const int J = 2044 - 8 * tid;
#pragma unroll
    for (int x = -7; x <= 3; ++x) {
      int j = J + x;
      u32 bits = pk_bf16((float)j, 0.f) & 0xffffu;
      int idx = j < 256 ? j : (int)bits - 17024;
      gidx[x + 7] = KV + (j < 0 ? 740 : idx + (idx >> 3)) * 4;
    }
  }
  const int q = lane >> 4, i16 = lane & 15;
  u32 erow;
  {
    int s = wave, b = i16;
#pragma unroll
    for (int k = 0; k < 3; ++k) if (s > 0 && b < 8) { --s; b *= 2; }
    erow = TABE + (s == 0 ? b : 8 * s + b) * RS + q * 16;
  }
  SCHED0;
  float par[3][4], dsk[3], wsc[3];
#pragma unroll
  for (int ch = 0; ch < 3; ++ch) {
    const float dt = rbf(expf(bf2f(rdt[ch])));
    dsk[ch] = bf2f(rds[ch]);
    const float ar = dt * bf2f(raw[ch][0]), ai = dt * bf2f(raw[ch][1]) * 0.15915494309189535f;
    float br = dt * bf2f(raw[ch][2]), bi = dt * bf2f(raw[ch][3]), cr = bf2f(raw[ch][4]), ci = bf2f(raw[ch][5]);
    const float wr = __builtin_fmaf(cr, br, -(ci * bi)), wi = __builtin_fmaf(cr, bi, ci * br);
    br = dt * bf2f(raw[ch][6]); bi = dt * bf2f(raw[ch][7]); cr = bf2f(raw[ch][8]); ci = bf2f(raw[ch][9]);
    const float wr2 = __builtin_fmaf(cr, br, -(ci * bi)), wi2 = __builtin_fmaf(cr, bi, ci * br);
    float m = fmaxf(fmaxf(fabsf(wr), fabsf(wi)), fmaxf(fabsf(wr2), fabsf(wi2)));
    m = wave_max_nonneg(m);
    int e = m > 0.f ? __builtin_amdgcn_frexp_expf(m) : 14;
    e = e < -100 ? -100 : e;
    wsc[ch] = __builtin_ldexpf(1.f, e - 14);
    const float sc = __builtin_ldexpf(1.f, 14 - e);
    par[ch][0] = half ? ar * 16.f : ar; par[ch][1] = half ? ai * -16.f : ai;
    par[ch][2] = half ? wr * sc : 1.f; par[ch][3] = half ? -wi * sc : 0.f;
  }
  S2V(1);
  const u32 tabw = (half ? 0 : TABE) + n * 4;
  const u32 xrow = ((wave == 0 ? 0 : 8 + 8 * wave) + i16) * RS + q * 16;
  u32 kvw[4];
  {
    const int nrow = wave == 0 ? 16 : wave == 3 ? 9 : 8, r0 = wave == 0 ? 0 : 8 + 8 * wave;
#pragma unroll
    for (int i = 0; i < 4; ++i) kvw[i] = KV + (4 * q + i < nrow ? (r0 + 4 * q + i) * 18 + i16 + (i16 >> 3) : 741) * 4;
  }
  if (tid < 3) *(LAS float*)(LAS char*)(smem + KV + tid * KVS + 740 * 4) = 0.f;
  for (int ch = -1; ch < 3; ++ch) {
    f32x4 kacc = (f32x4)(0.f), kacc2 = (f32x4)(0.f);
    if (ch >= 0) {
      f16x8 ka[8], kb_[8];
#pragma unroll
      for (int kb = 0; kb < 8; ++kb) {
        ka[kb] = *(const LAS f16x8*)(LAS char*)(smem + xrow + kb * 64);
        kb_[kb] = *(const LAS f16x8*)(LAS char*)(smem + erow + kb * 64);
      }
      WAITL0; BAR;
      if (ch == 0) S2V(3);
#pragma unroll
      for (int kb = 0; kb < 8; kb += 2) {
        kacc = __builtin_amdgcn_mfma_f32_16x16x32_f16(ka[kb], kb_[kb], kacc, 0, 0, 0);
        kacc2 = __builtin_amdgcn_mfma_f32_16x16x32_f16(ka[kb + 1], kb_[kb + 1], kacc2, 0, 0, 0);
      }
      SCHED0;
    }
    if (ch < 2) {
      const int c1 = ch + 1;
      const float ar = c1 == 0 ? par[0][0] : c1 == 1 ? par[1][0] : par[2][0];
      const float ai = c1 == 0 ? par[0][1] : c1 == 1 ? par[1][1] : par[2][1];
      const f32x2 W = {c1 == 0 ? par[0][2] : c1 == 1 ? par[1][2] : par[2][2], c1 == 0 ? par[0][3] : c1 == 1 ? par[1][3] : par[2][3]};
      CP P[8];
      P[0] = mkcp(cexp_(ar, ai));
#pragma unroll
      for (int i = 1; i < 4; ++i) P[i] = mkcp(cmul(P[i - 1].v, P[i - 1]));
      P[4] = mkcp(cexp_(ar * 16.f, ai * 16.f));
#pragma unroll
      for (int i = 5; i < 8; ++i) P[i] = mkcp(cmul(P[i - 1].v, P[i - 1]));
      f32x2 X[16];
      X[0] = W; X[1] = cmul(W, P[0]); X[2] = cmul(W, P[1]); X[3] = cmul(X[1], P[1]);
#pragma unroll
      for (int k = 0; k < 4; ++k) X[4 + k] = cmul(X[k], P[2]);
#pragma unroll
      for (int k = 0; k < 8; ++k) X[8 + k] = cmul(X[k], P[3]);
#pragma unroll
      for (int a = 0; a < 16; ++a) st_h2(smem, tabw + a * RS, X[a]);
#pragma unroll
      for (int s = 1; s < 4; ++s) {
        f32x2 Y[8];
        Y[0] = cmul(W, P[s + 3]); Y[1] = cmul(Y[0], P[s]); Y[2] = cmul(Y[0], P[s + 1]); Y[3] = cmul(Y[1], P[s + 1]);
#pragma unroll
        for (int k = 0; k < 4; ++k) Y[4 + k] = cmul(Y[k], P[s + 2]);
#pragma unroll
        for (int a = 0; a < 8; ++a) st_h2(smem, tabw + (16 + 8 * (s - 1) + a) * RS, Y[a]);
      }
      st_h2(smem, tabw + 40 * RS, cmul(W, P[7]));
    }
    if (ch >= 0) {
      SCHED0;
      const float ksc = ch == 0 ? wsc[0] : ch == 1 ? wsc[1] : wsc[2];
      kacc += kacc2;
#pragma unroll
      for (int i = 0; i < 4; ++i) *(LAS float*)(LAS char*)(smem + kvw[i] + ch * KVS) = kacc[i] * ksc;
    }
    WAITL0; BAR;
    if (ch == -1) S2V(2);
    if (ch == 0) S2V(4);
  }
  S2V(5);
  {
    float V[3][11];
#pragma unroll
    for (int ch = 0; ch < 3; ++ch)
#pragma unroll
      for (int x = 0; x < 11; ++x) V[ch][x] = *(const LAS float*)(LAS char*)(smem + gidx[x] + ch * KVS);
    SCHED0;
#pragma unroll
    for (int ch = 0; ch < 3; ++ch) {
      u32 P[11];
#pragma unroll
      for (int x = 1; x < 11; ++x) P[x] = pk_bf16(V[ch][x], V[ch][x - 1]);
#pragma unroll
      for (int sg = 0; sg < 4; ++sg) {
        u32x4 c = {P[sg + 7], P[sg + 5], P[sg + 3], P[sg + 1]};
        *(LAS u32x4*)(LAS char*)(smem + ch * CPY + sg * CPS + tid * 16) = c;
      }
    }
  }
  if (tid < 24) *(LAS u32x4*)(LAS char*)(smem + (tid >> 1) * CPS + 4096 + (tid & 1) * 16) = (u32x4)(0u);
  WAITV0; WAITL0; BAR;
  S2V(6);
  auto finish = [&](const f32x4 s, const u32x2 uu, int ch, int i) {
    const float dskc = ch == 0 ? dsk[0] : ch == 1 ? dsk[1] : dsk[2];
    const int t = 256 * i + 16 * i16 + 4 * q;
    float a0 = rbf(s[0]) + rbf(lo_f(uu[0]) * dskc), a1 = rbf(s[1]) + rbf(hi_f(uu[0]) * dskc);
    float a2 = rbf(s[2]) + rbf(lo_f(uu[1]) * dskc), a3 = rbf(s[3]) + rbf(hi_f(uu[1]) * dskc);
    u32x2 pv = {pk_bf16(a0, a1), pk_bf16(a2, a3)};
    *(LAS u32x2*)(LAS char*)(smem + US + ch * USTR + 480 + t * 2) = pv;
  };
  const u32 abase0 = (i16 & 3) * CPS + (2044 - 16 + 8 * q - 4 * (i16 >> 2)) * 2;
  const u32 bbase0 = US + 480 + (16 * i16 - 16 + 8 * q) * 2;
  if (wave < 3) {
    constexpr int NTL = 8;
    f32x4 Y[NTL];
#pragma unroll
    for (int i = 0; i < NTL; ++i) Y[i] = (f32x4)(0.f);
    const u32 abase = abase0 + wave * CPY, bbase = bbase0 + wave * USTR;
    bf16x8 A0[NTL], B0[NTL], A1[NTL], B1[NTL];
    auto mm = [&](const bf16x8 (&A)[NTL], const bf16x8 (&B)[NTL], bf16x8 (&An)[NTL], bf16x8 (&Bn)[NTL], int cn, bool doit, bool more) {
      const u32 aa = abase - cn * 64, bb = bbase - cn * 64;
      u32x2 lo[NTL], hi[NTL];
      int idx = 0;
      WAITL0; SCHED0;
#pragma unroll
      for (int k = 0; k < NTL; ++k)
#pragma unroll
        for (int pp = 0; pp + k < NTL; ++pp) {
          if (doit) Y[pp + k] = __builtin_amdgcn_mfma_f32_16x16x32_bf16(A[pp], B[k], Y[pp + k], 0, 0, 0);
          if (more) {
            if (idx < NTL) Bn[idx] = *(const LAS bf16x8*)(LAS char*)(smem + bb + idx * 512);
            else if (idx < 2 * NTL) lo[idx - NTL] = *(const LAS u32x2*)(LAS char*)(smem + aa - (idx - NTL) * 512);
            else if (idx < 3 * NTL) hi[idx - 2 * NTL] = *(const LAS u32x2*)(LAS char*)(smem + aa - (idx - 2 * NTL) * 512 + 8);
          }
          ++idx;
          SCHED0;
        }
      if (more) {
#pragma unroll
        for (int pp = 0; pp < NTL; ++pp) An[pp] = __builtin_bit_cast(bf16x8, (u32x4){lo[pp][0], lo[pp][1], hi[pp][0], hi[pp][1]});
      }
    };
    mm(A1, B1, A0, B0, 0, false, true);
    for (int c = 0; c < 8; c += 2) {
      mm(A0, B0, A1, B1, c + 1, true, true);
      mm(A1, B1, A0, B0, c + 2, true, c + 2 < 8);
    }
    S2V(7);
    u32x2 uu[NTL];
#pragma unroll
    for (int i = 0; i < NTL; ++i) uu[i] = *(const LAS u32x2*)(LAS char*)(smem + US + wave * USTR + 480 + (256 * i + 16 * i16 + 4 * q) * 2);
    SCHED0;
#pragma unroll
    for (int i = 0; i < NTL; ++i) finish(Y[i], uu[i], wave, i);
    WAITL0;
    {
      const rsrc_t ra = mk_rsrc(ws + WS_AT + (size_t)(d0 + wave) * 4096, 4096);
#pragma unroll
      for (int k = 0; k < 4; ++k) {
        const u32x4 v = *(const LAS u32x4*)(LAS char*)(smem + US + wave * USTR + 480 + (k * 64 + lane) * 16);
        __builtin_amdgcn_raw_buffer_store_b128(v, ra, (k * 64 + lane) * 16, 0, AUX_SC1);
      }
    }
    S2V(8);
  }
  WAITV0; BAR;
  S2V(10); S2R(15);
  if (tid < 4) __hip_atomic_fetch_add((u32*)(ws + WS_CNT + ((d0 / 72) * 4 + tid) * 256), 1u, __ATOMIC_RELAXED, __HIP_MEMORY_SCOPE_AGENT);
}

__global__ __launch_bounds__(256, 2) void k2_gemm2_conv(const u16* __restrict__ are, const u16* __restrict__ aim,
    const u16* __restrict__ bre, const u16* __restrict__ bim, const u16* __restrict__ cre, const u16* __restrict__ cim,
    const u16* __restrict__ logdt, const u16* __restrict__ dskip, char* __restrict__ ws, u16* __restrict__ out) {
  extern __shared__ __attribute__((aligned(16))) char smem[];
  const int b = blockIdx.x;
  const int wave = __builtin_amdgcn_readfirstlane(threadIdx.x >> 6), lane = threadIdx.x & 63;
  if (b < NCONV) conv_role(smem, are, aim, bre, bim, cre, cim, logdt, dskip, ws, b, wave, lane);
  else { const int g = b - NCONV; gemm2_role(smem, ws, out, g & 7, g >> 3, wave, lane); }
}

extern "C" void kernel_launch(void* const* d_in, const int* in_sizes, int n_in, void* d_out, int out_size, void* d_ws, size_t ws_size, hipStream_t stream) {
  (void)hipFuncSetAttribute((const void*)k1_gemm1, hipFuncAttributeMaxDynamicSharedMemorySize, K1_LDS);
  (void)hipFuncSetAttribute((const void*)k2_gemm2_conv, hipFuncAttributeMaxDynamicSharedMemorySize, K2_LDS);
  k1_gemm1<<<256, 512, K1_LDS, stream>>>((const u16*)d_in[0], (const u16*)d_in[10], (const u16*)d_in[11], (const u16*)d_in[12], (const u16*)d_in[13], (char*)d_ws,
      (const u16*)d_in[1], (const u16*)d_in[2], (const u16*)d_in[3], (const u16*)d_in[4], (const u16*)d_in[5], (const u16*)d_in[6]);
  k2_gemm2_conv<<<NCONV + 256, 256, K2_LDS, stream>>>((const u16*)d_in[1], (const u16*)d_in[2], (const u16*)d_in[3], (const u16*)d_in[4],
      (const u16*)d_in[5], (const u16*)d_in[6], (const u16*)d_in[7], (const u16*)d_in[8], (char*)d_ws, (u16*)d_out);
}
```

```cpp
#include <hip/hip_runtime.h>
typedef unsigned short u16;
typedef unsigned int u32;
typedef short s16x4 __attribute__((ext_vector_type(4)));
typedef __bf16 bf16x8 __attribute__((ext_vector_type(8)));
typedef __bf16 bf16x2 __attribute__((ext_vector_type(2)));
typedef _Float16 f16x2 __attribute__((ext_vector_type(2)));
typedef _Float16 f16x8 __attribute__((ext_vector_type(8)));
typedef float f32x2 __attribute__((ext_vector_type(2)));
typedef float f32x4 __attribute__((ext_vector_type(4)));
typedef float f32x16 __attribute__((ext_vector_type(16)));
typedef u32 u32x2 __attribute__((ext_vector_type(2)));
typedef u32 u32x4 __attribute__((ext_vector_type(4)));
#define LAS __attribute__((address_space(3)))
#define GAS __attribute__((address_space(1)))

constexpr int L_ = 2048, D_ = 576, NS_ = 128, F_ = 4608;
constexpr size_t WS_H   = 0;
constexpr size_t WS_WF  = WS_H + (size_t)32 * 144 * 4096;
constexpr size_t WS_UT  = WS_WF + (size_t)8 * 144 * 4608;
constexpr size_t WS_AT  = WS_UT + (size_t)D_ * L_ * 2;
constexpr size_t WS_G   = WS_AT + (size_t)D_ * L_ * 2;
constexpr size_t WS_CNT = WS_G + (size_t)L_ * 4;
constexpr size_t WS_DBG = 48u << 20;

__device__ __forceinline__ float bf2f(u16 v) { return __uint_as_float(((u32)v) << 16); }
__device__ __forceinline__ u32 pk_bf16(float lo, float hi) {
  f32x2 v = {lo, hi}; bf16x2 b = __builtin_convertvector(v, bf16x2); return __builtin_bit_cast(u32, b);
}
__device__ __forceinline__ float lo_f(u32 p) { return __uint_as_float(p << 16); }
__device__ __forceinline__ float hi_f(u32 p) { return __uint_as_float(p & 0xffff0000u); }
__device__ __forceinline__ void mul_halves8(u32 (&q)[8]) {
  asm("v_dot2_f32_bf16 %0, %0, %8, 0\n\tv_dot2_f32_bf16 %1, %1, %9, 0\n\tv_dot2_f32_bf16 %2, %2, %10, 0\n\tv_dot2_f32_bf16 %3, %3, %11, 0\n\t"
      "v_dot2_f32_bf16 %4, %4, %12, 0\n\tv_dot2_f32_bf16 %5, %5, %13, 0\n\tv_dot2_f32_bf16 %6, %6, %14, 0\n\tv_dot2_f32_bf16 %7, %7, %15, 0\n\ts_nop 2"
      : "+v"(q[0]), "+v"(q[1]), "+v"(q[2]), "+v"(q[3]), "+v"(q[4]), "+v"(q[5]), "+v"(q[6]), "+v"(q[7])
      : "v"(q[0] >> 16), "v"(q[1] >> 16), "v"(q[2] >> 16), "v"(q[3] >> 16), "v"(q[4] >> 16), "v"(q[5] >> 16), "v"(q[6] >> 16), "v"(q[7] >> 16));
}
__device__ __forceinline__ float rbf(float x) { return lo_f(pk_bf16(x, 0.f)); }

__device__ __forceinline__ void glds16(const void* g, u32 lds_byte, char* smem) {
  __builtin_amdgcn_global_load_lds((const GAS void*)g, (LAS void*)(smem + lds_byte), 16, 0, 0);
}
typedef __amdgpu_buffer_rsrc_t rsrc_t;
__device__ __forceinline__ rsrc_t mk_rsrc(const void* p, u32 bytes) {
  return __builtin_amdgcn_make_buffer_rsrc((void*)p, 0, bytes, 0x00020000);
}
__device__ __forceinline__ void blds16(rsrc_t r, u32 voff, u32 soff, u32 lds_byte, char* smem) {
  __builtin_amdgcn_raw_ptr_buffer_load_lds(r, (LAS void*)(smem + lds_byte), 16, voff, soff, 0, 0);
}
__device__ __forceinline__ s16x4 tr_read(char* smem, u32 off) {
  return __builtin_amdgcn_ds_read_tr16_b64_v4i16((LAS s16x4*)(smem + off));
}
#define WAITV0 asm volatile("s_waitcnt vmcnt(0)" ::: "memory")
#define WAITL0 asm volatile("s_waitcnt lgkmcnt(0)" ::: "memory")
#define BAR __builtin_amdgcn_s_barrier()
#define SCHED0 __builtin_amdgcn_sched_barrier(0)

#define DMA_EVERY 2
#define DMA_N0 4
#define DMA_N1 4
constexpr int X0_ = 0, XST = 32768, W0_ = 65536, WST = 36992, UOFF = 18432 + 128;
constexpr int WDN = X0_ + XST;
constexpr int UTS = W0_ + 2 * WST;
constexpr int GWL = UTS + 9216;
constexpr int K1_LDS = GWL + 1152;

template <int NT>
struct Frag { bf16x8 x[2]; bf16x8 w[NT]; };

template <int NT>
__device__ __forceinline__ void k1_load_frags(Frag<NT>& f, char* smem, u32 xaddr, u32 waddr, int stage, int s) {
#pragma unroll
  for (int xi = 0; xi < 2; ++xi)
    f.x[xi] = *(const LAS bf16x8*)(LAS char*)(smem + xaddr + stage * XST + xi * 4096);
#pragma unroll
  for (int T = 0; T < NT; ++T) {
    s16x4 a = tr_read(smem, waddr + stage * WST + (16 * s) * 288 + 32 * T);
    s16x4 b = tr_read(smem, waddr + stage * WST + (16 * s + 4) * 288 + 32 * T);
    typedef short s16x8 __attribute__((ext_vector_type(8)));
    s16x8 c = __builtin_shufflevector(a, b, 0, 1, 2, 3, 4, 5, 6, 7);
    f.w[T] = __builtin_bit_cast(bf16x8, c);
  }
}
template <int NT>
__device__ __forceinline__ void k1_mma(f32x16 (&acc)[NT][2], const Frag<NT>& f) {
#pragma unroll
  for (int T = 0; T < NT; ++T)
#pragma unroll
    for (int xi = 0; xi < 2; ++xi)
      acc[T][xi] = __builtin_amdgcn_mfma_f32_32x32x16_bf16(f.w[T], f.x[xi], acc[T][xi], 0, 0, 0);
}
template <int NT, int NDMA>
__device__ __forceinline__ void k1_interleave() {
#pragma unroll
  for (int i = 0; i < NT * 2; ++i) {
    __builtin_amdgcn_sched_group_barrier(0x8, 1, 0);
    if (i < 2) __builtin_amdgcn_sched_group_barrier(0x100, 2, 0); else __builtin_amdgcn_sched_group_barrier(0x100, 1, 0);
    if (i < NDMA) __builtin_amdgcn_sched_group_barrier(0x10, 1, 0);
  }
}

template <int NT, int WN>
__device__ __forceinline__ void k1_body(char* smem, const u16* __restrict__ x, const u16* __restrict__ wg, const u16* __restrict__ wu,
                                        const u16* __restrict__ wd, const u16* __restrict__ gw, char* __restrict__ ws, int ib, int jb, int wave, int lane,
                                        const u16* p0, const u16* p1, const u16* p2, const u16* p3, const u16* p4, const u16* p5, const u16* outp) {
  constexpr int NW = (WN == 0) ? 4 : 5;
  const int wm = wave & 3;
  u32 xoff[4], woff[NW];
#pragma unroll
  for (int ii = 0; ii < 4; ++ii) {
    int rr = lane >> 3, row = 32 * wave + 8 * ii + rr;
    int c = (lane & 7) ^ ((row >> 1) & 7);
    xoff[ii] = row * 1152 + c * 16;
  }
  const int wi0 = (WN == 0) ? 4 * wave : 16 + 5 * (wave - 4);
#pragma unroll
  for (int k = 0; k < NW; ++k) {
    int wi = wi0 + k; int gi = wi >= 18 ? wi - 18 : wi;
    int ci = 64 * gi + lane, row = ci / 18, w18 = ci - row * 18;
    int colb = (w18 < 16) ? (256 * jb + 16 * w18) : (8192 + 32 * jb + 16 * (w18 - 16));
    woff[k] = row * 9216 + colb;
  }
  const rsrc_t rx = mk_rsrc((const char*)x + (size_t)ib * 256 * 1152, 256 * 1152);
  const rsrc_t rg = mk_rsrc(wg, D_ * F_ * 2), ru = mk_rsrc(wu, D_ * F_ * 2);
  const int r32 = lane & 31, hh = lane >> 5;
  u32 xa[4];
#pragma unroll
  for (int s = 0; s < 4; ++s) xa[s] = X0_ + (64 * wm + r32) * 128 + (((2 * s + hh) ^ ((r32 >> 1) & 7)) * 16);
  const int g4 = lane >> 4;
  const u32 wa = W0_ + ((g4 & 1) ? UOFF : 0) + (8 * (g4 >> 1) + ((lane & 15) >> 2)) * 288 + (lane & 3) * 8 + (WN ? 5 * 32 : 0);

  f32x16 acc[NT][2];
#pragma unroll
  for (int T = 0; T < NT; ++T) { acc[T][0] = (f32x16)(0.f); acc[T][1] = (f32x16)(0.f); }
  Frag<NT> f0, f1;
  const int lane_ = lane;

  auto dma1 = [&](int t, int stage, int d) {
    if (d < 4) blds16(rx, xoff[d], t * 128, X0_ + stage * XST + (32 * wave + 8 * d) * 128, smem);
    else {
      const int k = d - 4; int wi = wi0 + k; bool up = wi >= 18; int gi = up ? wi - 18 : wi;
      blds16(up ? ru : rg, woff[k], t * 64 * 9216, W0_ + stage * WST + (up ? UOFF : 0) + gi * 1024, smem);
    }
  };
  float gs0 = 0.f, gs1 = 0.f;
  u32 ux0 = 0, ux1 = 0, ugw = 0;
  const int urow = 8 * jb + 2 * (wave - 4);
  const u32 uaddr = X0_ + urow * 128 + (((lane >> 3) ^ ((urow >> 1) & 7)) << 4) + (lane & 7) * 2;
  auto sub = [&](Frag<NT>& cur, Frag<NT>& nxt, bool do_mma, int stage, int s, int t1, int d0, int nd) {
    typedef short s16x8 __attribute__((ext_vector_type(8)));
    s16x4 ta[NT], tb[NT];
    int di = 0;
#pragma unroll
    for (int i = 0; i < 2 * NT; ++i) {
      if (do_mma) acc[i >> 1][i & 1] = __builtin_amdgcn_mfma_f32_32x32x16_bf16(cur.w[i >> 1], cur.x[i & 1], acc[i >> 1][i & 1], 0, 0, 0);
      if (i == 0) {
        nxt.x[0] = *(const LAS bf16x8*)(LAS char*)(smem + xa[s] + stage * XST);
        nxt.x[1] = *(const LAS bf16x8*)(LAS char*)(smem + xa[s] + stage * XST + 4096);
      } else if (i == 1) {
        ta[0] = tr_read(smem, wa + stage * WST + (16 * s) * 288);
        tb[0] = tr_read(smem, wa + stage * WST + (16 * s + 4) * 288);
      } else {
        const int T = i >> 1;
        if ((i & 1) == 0) ta[T] = tr_read(smem, wa + stage * WST + (16 * s) * 288 + 32 * T);
        else              tb[T] = tr_read(smem, wa + stage * WST + (16 * s + 4) * 288 + 32 * T);
      }
      if (WN == 1) {
        if (s == 0 && i == 7) {
          ux0 = *(const LAS u16*)(LAS char*)(smem + uaddr + stage * XST);
          ux1 = *(const LAS u16*)(LAS char*)(smem + uaddr + stage * XST + 128);
          ugw = *(const LAS u16*)(LAS char*)(smem + GWL + ((t1 - 1) * 64 + lane) * 2);
        }
        if (s == 1 && i == 4) gs0 += rbf(__uint_as_float(ux0 << 16) * __uint_as_float(ugw << 16));
        if (s == 1 && i == 5) gs1 += rbf(__uint_as_float(ux1 << 16) * __uint_as_float(ugw << 16));
        if (s == 1 && i == 6) *(LAS u32*)(LAS char*)(smem + UTS + ((t1 - 1) * 64 + lane) * 16 + (wave - 4) * 4) = ux0 | (ux1 << 16);
      }
      if (di < nd && (i % DMA_EVERY) == (WN ? 1 : 2) % DMA_EVERY) { dma1(t1, stage ^ 1, d0 + di); ++di; }
      SCHED0;
    }
#pragma unroll
    for (int k = 0; k < 9; ++k) if (di < nd) { dma1(t1, stage ^ 1, d0 + di); ++di; }
#pragma unroll
    for (int T = 0; T < NT; ++T) nxt.w[T] = __builtin_bit_cast(bf16x8, __builtin_shufflevector(ta[T], tb[T], 0, 1, 2, 3, 4, 5, 6, 7));
    SCHED0;
  };
  auto wrelay = [&]() {
    int lane = lane_; asm volatile("" : "+v"(lane));
    const int g4 = lane >> 4, i16 = lane & 15, hh2 = g4 & 1;
#pragma unroll
    for (int r = 0; r < 3; ++r) {
      const int item = 2 * (wave + 8 * r) + (g4 >> 1);
      if (wave + 8 * r < 23) {
        const int it = item < 45 ? item : 44, T = it / 5, m = it - 5 * T;
        const u32 la = WDN + (16 * T + 4 * hh2 + (i16 >> 2)) * 144 + m * 32 + (i16 & 3) * 8;
        s16x4 a = tr_read(smem, la), b = tr_read(smem, la + 8 * 144);
        typedef short s16x8 __attribute__((ext_vector_type(8)));
        const u32x4 v = __builtin_bit_cast(u32x4, __builtin_shufflevector(a, b, 0, 1, 2, 3, 4, 5, 6, 7));
        const int G = 9 * jb + T, qq = 2 * (G & 1) + hh2;
        char* dst = ws + WS_WF + (size_t)(ib * 144 + (G >> 1)) * 4608 + (m < 4 ? m * 1024 + (qq * 16 + i16) * 16 : 4096 + (qq * 8 + i16) * 16);
        if (item < 45 && (m < 4 || i16 < 8)) *(u32x4*)dst = v;
      }
    }
  };
  u32 pfd = 0;
  u32 pfo;
  if (WN == 0) { const int l2 = (ib & 3) * 48 + wave * 12 + lane, row = l2 / 3, c = l2 - 3 * row; pfo = row * 9216 + (c < 2 ? 256 * jb + 128 * c : 8192 + 32 * jb); }
  else pfo = ((jb & 3) * 64 + (wave - 4) * 16 + lane) * 1152;
  const rsrc_t rpf = WN == 0 ? mk_rsrc(ib >= 4 ? wu : wg, D_ * F_ * 2) : rx;
  auto pf = [&](int tp) {
    if (tp <= 8) {
      const u32 so = WN == 0 ? tp * 64 * 9216 : tp * 128;
      if (lane < (WN == 0 ? 12 : 16)) asm volatile("buffer_load_dword %0, %1, %2, %3 offen" : "+v"(pfd) : "v"(pfo), "s"(rpf), "s"(so) : "memory");
    } else {
      int ln = lane_; asm volatile("" : "+v"(ln));
      const int li = min(wave * 36 + ln, 287), row = li >> 1, T = row >> 4;
      const u32 o2 = ((T < 8 ? 128 * jb + 16 * T : 4096 + 16 * jb) + (row & 15)) * 1152 + ib * 144 + (li & 1) * 128;
      const rsrc_t rd = mk_rsrc(wd, F_ * D_ * 2);
      if (ln < 36) asm volatile("buffer_load_dword %0, %1, %2, 0 offen" : "+v"(pfd) : "v"(min(o2, (u32)(F_ * D_ * 2 - 4))), "s"(rd) : "memory");
    }
  };
  auto wdn_dma = [&]() {
    int lane = lane_; asm volatile("" : "+v"(lane));
    const rsrc_t rd = mk_rsrc((const char*)wd + ib * 144, F_ * D_ * 2 - ib * 144);
#pragma unroll
    for (int r = 0; r < 3; ++r) {
      const int k = wave + 8 * r, ci = 64 * k + lane;
      if (k < 21) {
        if (ci < 1296) {
          const int row = ci / 9, cc = ci - 9 * row, T = row >> 4;
          const int f = (T < 8 ? 128 * jb + 16 * T : 4096 + 16 * jb) + (row & 15);
          blds16(rd, f * 1152 + cc * 16, 0, WDN + k * 1024, smem);
        }
      }
    }
  };
  auto step = [&](int t, int stage, bool first, bool last) {
    {
      int ne = 0;
      ne += first ? 2 : (t - 1 <= 6 ? 1 : 0);
      ne += first ? 1 : 0;
      if (ne == 0) WAITV0;
      else if (ne == 1) asm volatile("s_waitcnt vmcnt(1)" ::: "memory");
      else if (ne == 2) asm volatile("s_waitcnt vmcnt(2)" ::: "memory");
      else if (ne == 3) asm volatile("s_waitcnt vmcnt(3)" ::: "memory");
      else asm volatile("s_waitcnt vmcnt(4)" ::: "memory");
    }
    WAITL0; BAR; SCHED0;
    if (last) { wdn_dma(); SCHED0; }
    constexpr int ND = 4 + NW;
    constexpr int N0 = DMA_N0, N1 = (ND - N0 < DMA_N1) ? ND - N0 : DMA_N1, N2 = ND - N0 - N1;
    sub(f1, f0, !first, stage, 0, t + 1, 0, last ? 0 : N0);
    sub(f0, f1, true, stage, 1, t + 1, N0, last ? 0 : N1);
    sub(f1, f0, true, stage, 2, t + 1, N0 + N1, last ? 0 : N2);
    SCHED0;
    if (t <= 6) pf(t + 3);
    SCHED0;
    sub(f0, f1, true, stage, 3, t + 1, 0, 0);
  };
  if (wave == 4) {
    const rsrc_t rgw = mk_rsrc(gw, D_ * 2);
    blds16(rgw, lane * 16, 0, GWL, smem);
    if (lane < 8) blds16(rgw, 1024 + lane * 16, 0, GWL + 1024, smem);
  }
#pragma unroll
  for (int p = 0; p < 4 + NW; ++p) dma1(0, 0, p);
pf(1); pf(2);
  {
    const int g = (int)(blockIdx.x >> 3) * 16 + wave * 2 + (lane & 1);
    const char* pa = lane < 2 ? ws + (size_t)min(g, (int)(WS_CNT >> 16)) * 65536 : (const char*)outp + (size_t)min(g, 35) * 65536;
    if (lane < 2 || (lane < 4 && g < 36)) asm volatile("global_load_dword %0, %1, off" : "+v"(pfd) : "v"(pa) : "memory");
  }
  step(0, 0, true, false);
  for (int t = 1; t < 7; t += 2) { step(t, 1, false, false); step(t + 1, 0, false, false); }
  step(7, 1, false, false);
  step(8, 0, false, true);
  k1_mma<NT>(acc, f1);
  WAITV0; SCHED0;
  asm volatile("" :: "v"(pfd));
  if (WN == 0 && wave == 0) {
    int ln = lane_; asm volatile("" : "+v"(ln));
    const int a = min(ln / 6, 5), c = ln % 6;
    const u16* pa = a == 0 ? p0 : a == 1 ? p1 : a == 2 ? p2 : a == 3 ? p3 : a == 4 ? p4 : p5;
    if (blockIdx.x < 192 && ln < 36) asm volatile("global_load_dword %0, %1, off" : "+v"(pfd) : "v"(pa + blockIdx.x * 384 + c * 64) : "memory");
  }
  if (WN == 1) {
#pragma unroll
    for (int o2 = 32; o2 > 0; o2 >>= 1) { gs0 += __shfl_xor(gs0, o2); gs1 += __shfl_xor(gs1, o2); }
    const float v = rbf(lane == 0 ? gs0 : gs1);
    if (lane < 2) ((float*)(ws + WS_G))[256 * ib + urow + lane] = rbf(1.f / (1.f + expf(-v)));
    if (blockIdx.x == 0 && wave == 4 && lane < 32) *(u32*)(ws + WS_CNT + lane * 256) = 0u;
  }

  char* hb = ws + WS_H + (size_t)(4 * ib + wm) * 144 * 4096 + ((r32 >> 4) * 1024 + (hh * 16 + (r32 & 15)) * 16);
#pragma unroll
  for (int T = 0; T < NT; ++T) {
    const int G = 9 * jb + T + (WN ? 5 : 0);
#pragma unroll
    for (int xi = 0; xi < 2; ++xi) {
      u32x4 o;
      u32 qq[8];
#pragma unroll
      for (int i = 0; i < 8; ++i) qq[i] = pk_bf16(acc[T][xi][i], acc[T][xi][8 + i]);
      mul_halves8(qq);
#pragma unroll
      for (int i = 0; i < 4; ++i) o[i] = pk_bf16(__uint_as_float(qq[2 * i]), __uint_as_float(qq[2 * i + 1]));
      *(u32x4*)(hb + (size_t)(G >> 1) * 4096 + xi * 2048 + (G & 1) * 512) = o;
    }
  }
  WAITL0; BAR; SCHED0;
  wrelay();
  {
    const int tid = wave * 64 + lane;
    char* ut = ws + WS_UT + (256 * ib + 8 * jb) * 2;
    *(u32x4*)(ut + (size_t)tid * 4096) = *(const LAS u32x4*)(LAS char*)(smem + UTS + tid * 16);
    if (tid < 64) *(u32x4*)(ut + (size_t)(512 + tid) * 4096) = *(const LAS u32x4*)(LAS char*)(smem + UTS + (512 + tid) * 16);
  }
}

__global__ __launch_bounds__(512, 2) void k1_gemm1(const u16* __restrict__ x, const u16* __restrict__ wg, const u16* __restrict__ wu,
                                                   const u16* __restrict__ wd, const u16* __restrict__ gw, char* __restrict__ ws,
                                                   const u16* p0, const u16* p1, const u16* p2, const u16* p3, const u16* p4, const u16* p5, const u16* outp) {
  extern __shared__ __attribute__((aligned(16))) char smem[];
  const int b = blockIdx.x, idx = b >> 3;
  const int ib = idx & 7, jb = 4 * (b & 7) + (idx >> 3);
  const int wave = __builtin_amdgcn_readfirstlane(threadIdx.x >> 6), lane = threadIdx.x & 63;
  if (wave < 4) k1_body<5, 0>(smem, x, wg, wu, wd, gw, ws, ib, jb, wave, lane, p0, p1, p2, p3, p4, p5, outp);
  else          k1_body<4, 1>(smem, x, wg, wu, wd, gw, ws, ib, jb, wave, lane, p0, p1, p2, p3, p4, p5, outp);
}

#define S2V(i)
#define S2R(i)
#define S2ID
constexpr int K2_LDS = 72672;
constexpr int NCONV = 192;
constexpr u32 AUX_SC1 = 16;

__device__ __forceinline__ u32x4 bload16(rsrc_t r, u32 voff, u32 soff) {
  return __builtin_bit_cast(u32x4, __builtin_amdgcn_raw_buffer_load_b128(r, voff, soff, 0));
}
__device__ __forceinline__ int gbase(int G) {
  int jb = G / 9, T = G - 9 * jb;
  return T < 8 ? 128 * jb + 16 * T : 4096 + 16 * jb;
}

#define G2D 2
__device__ __forceinline__ void gemm2_role(char* smem, char* __restrict__ ws, u16* __restrict__ out,
                                           int ib, int idx, int wave, int lane) {
  const int rowsub = idx & 3, cb = idx >> 2, rb64 = 4 * ib + rowsub;
  const rsrc_t rh = mk_rsrc(ws + WS_H + (size_t)rb64 * 144 * 4096, 144 * 4096);
  const rsrc_t rw = mk_rsrc(ws + WS_WF + (size_t)cb * 144 * 4608, 144 * 4608);
  const int q = lane >> 4, i16 = lane & 15;
  f32x4 acc[5][4];
#pragma unroll
  for (int a = 0; a < 5; ++a)
#pragma unroll
    for (int b = 0; b < 4; ++b) acc[a][b] = (f32x4)(0.f);
  u32x4 hb[G2D][4], wb[G2D][5];
  const u32 hv = lane * 16, hv4 = 4096 + (q * 8 + (i16 & 7)) * 16;
  auto ldh = [&](int j, int r, int ks) { hb[j][r] = bload16(rh, hv + r * 1024, ks * 4096); };
  auto ldw = [&](int j, int m, int ks) { wb[j][m] = m < 4 ? bload16(rw, hv + m * 1024, ks * 4608) : bload16(rw, hv4, ks * 4608); };
  auto ldall = [&](int j, int ks) {
#pragma unroll
    for (int r = 0; r < 3; ++r) ldh(j, r, ks);
#pragma unroll
    for (int m = 0; m < 5; ++m) ldw(j, m, ks);
    ldh(j, 3, ks);
    SCHED0;
  };
  auto kstep = [&](int j, int ksn, bool more) {
#pragma unroll
    for (int r = 0; r < 4; ++r) {
#pragma unroll
      for (int m = 0; m < 5; ++m) {
        acc[m][r] = __builtin_amdgcn_mfma_f32_16x16x32_bf16(__builtin_bit_cast(bf16x8, wb[j][m]), __builtin_bit_cast(bf16x8, hb[j][r]), acc[m][r], 0, 0, 0);
        if (r == 3 && more) { SCHED0; ldw(j, m, ksn); SCHED0; }
      }
      SCHED0;
      if (more) ldh(j, r, ksn);
      SCHED0;
    }
  };
  S2V(0); S2R(14); S2ID;
#pragma unroll
  for (int j = 0; j < G2D; ++j) ldall(j, wave + 4 * j);
  SCHED0;
  constexpr int NIT = 36 / G2D;
  const u32* cnt = (const u32*)(ws + WS_CNT + (cb * 4 + rowsub) * 256);
  u32 early = 0;
  for (int it = 0; it < NIT - 1; ++it) {
    if (it == NIT - 2) early = __hip_atomic_load(cnt, __ATOMIC_RELAXED, __HIP_MEMORY_SCOPE_AGENT);
#pragma unroll
    for (int j = 0; j < G2D; ++j) kstep(j, wave + 4 * (G2D * (it + 1) + j), true);
    if (it == 0) S2V(1);
  }
  S2V(2);
  const int tid = wave * 64 + lane;
  u32x4 av[3];
  const float gv = ((const float*)(ws + WS_G))[rb64 * 64 + 16 * wave + i16];
  const rsrc_t ra = mk_rsrc(ws + WS_AT + (size_t)cb * 72 * 4096 + rb64 * 128, 72 * 4096);
  auto lda = [&]() {
#pragma unroll
    for (int k = 0; k < 3; ++k) {
      const int ci = min(tid + 256 * k, 575);
      av[k] = __builtin_bit_cast(u32x4, __builtin_amdgcn_raw_buffer_load_b128(ra, (ci >> 3) * 4096 + (ci & 7) * 16, 0, AUX_SC1));
    }
  };
  const bool ready = __builtin_amdgcn_readfirstlane(early) >= NCONV / 8;
  if (ready) lda();
  SCHED0;
  S2V(3);
#pragma unroll
  for (int j = 0; j < G2D; ++j) kstep(j, 0, false);
  S2V(4);

#pragma unroll
  for (int r = 0; r < 4; ++r) {
    if (r != wave) {
      const int slot = (wave - r - 1) & 3;
#pragma unroll
      for (int dt = 0; dt < 5; ++dt)
        *(LAS f32x4*)(LAS char*)(smem + ((r * 3 + slot) * 5 + dt) * 1024 + lane * 16) = acc[dt][r];
    }
  }
  f32x4 own[5];
#pragma unroll
  for (int dt = 0; dt < 5; ++dt) own[dt] = wave == 0 ? acc[dt][0] : wave == 1 ? acc[dt][1] : wave == 2 ? acc[dt][2] : acc[dt][3];
  if (lane == 0) *(LAS u32*)(LAS char*)(smem + 71808 + wave * 4) = ready ? 1u : 0u;
  WAITL0; BAR; asm volatile("" ::: "memory");
  const u32x4 rdy4 = *(const LAS u32x4*)(LAS char*)(smem + 71808);
  const bool allready = __builtin_amdgcn_readfirstlane(rdy4[0] & rdy4[1] & rdy4[2] & rdy4[3]) != 0;
  const float omg = rbf(1.f - gv);
#pragma unroll
  for (int dt = 0; dt < 5; ++dt) {
#pragma unroll
    for (int sl = 0; sl < 3; ++sl) own[dt] += *(const LAS f32x4*)(LAS char*)(smem + ((wave * 3 + sl) * 5 + dt) * 1024 + lane * 16);
#pragma unroll
    for (int i = 0; i < 4; ++i) own[dt][i] = rbf(omg * rbf(own[dt][i]));
  }
  S2V(5);
  if (!allready) {
    if (wave == 0) {
      for (int it = 0; it < (1 << 22); ++it) {
        if (__hip_atomic_load(cnt, __ATOMIC_RELAXED, __HIP_MEMORY_SCOPE_AGENT) >= NCONV / 8) break;
        __builtin_amdgcn_s_sleep(1);
      }
    }
    BAR; asm volatile("" ::: "memory");
    if (!ready) lda();
  }
#pragma unroll
  for (int k = 0; k < 3; ++k) {
    const int ci = tid + 256 * k;
    if (ci < 576) *(LAS u32x4*)(LAS char*)(smem + 61440 + (ci >> 3) * 144 + (ci & 7) * 16) = av[k];
  }
  WAITL0; BAR; asm volatile("" ::: "memory");
  S2V(8);
  u16* orow = out + (size_t)(rb64 * 64 + 16 * wave + i16) * D_ + cb * 72 + 4 * q;
#pragma unroll
  for (int dt = 0; dt < 5; ++dt) {
    if (dt < 4 || q < 2) {
      float o[4];
#pragma unroll
      for (int i = 0; i < 4; ++i) {
        float av = bf2f(*(const LAS u16*)(LAS char*)(smem + 61440 + (16 * dt + 4 * q + i) * 144 + (16 * wave + i16) * 2));
        o[i] = rbf(gv * av) + own[dt][i];
      }
      u32x2 pv = {pk_bf16(o[0], o[1]), pk_bf16(o[2], o[3])};
      *(u32x2*)(orow + 16 * dt) = pv;
    }
  }
  S2V(6);
  S2V(7); S2R(15);
}

constexpr int RS = 528, TABE = 41 * RS, CPS = 4160, CPY = 4 * CPS, KV = 3 * CPY, KVS = 2976, US = KV + 3 * KVS, USTR = 4608;
struct CP { f32x2 v, r; };
__device__ __forceinline__ CP mkcp(f32x2 p) { CP c; c.v = p; c.r = (f32x2){-p.y, p.x}; return c; }
__device__ __forceinline__ f32x2 cmul(f32x2 a, const CP& p) {
  return __builtin_elementwise_fma(p.r, (f32x2){a.y, a.y}, p.v * (f32x2){a.x, a.x});
}
__device__ __forceinline__ f32x2 cexp_(float ar, float air) {
  float e = __builtin_amdgcn_exp2f(ar * 1.44269504088896341f);
  float fr = __builtin_amdgcn_fractf(air);
  return (f32x2){e * __builtin_amdgcn_cosf(fr), e * __builtin_amdgcn_sinf(fr)};
}
__device__ __forceinline__ float wave_max_nonneg(float m) {
  int v = __float_as_int(m);
  v = max(v, __builtin_amdgcn_update_dpp(0, v, 0x111, 0xf, 0xf, true));
  v = max(v, __builtin_amdgcn_update_dpp(0, v, 0x112, 0xf, 0xf, true));
  v = max(v, __builtin_amdgcn_update_dpp(0, v, 0x114, 0xf, 0xf, true));
  v = max(v, __builtin_amdgcn_update_dpp(0, v, 0x118, 0xf, 0xf, true));
  const int a = __builtin_amdgcn_readlane(v, 15), b = __builtin_amdgcn_readlane(v, 31);
  const int c = __builtin_amdgcn_readlane(v, 47), d = __builtin_amdgcn_readlane(v, 63);
  return __int_as_float(max(max(a, b), max(c, d)));
}
__device__ __forceinline__ void st_h2(char* smem, u32 off, f32x2 v) {
  *(LAS f16x2*)(LAS char*)(smem + off) = __builtin_convertvector(v, f16x2);
}

__device__ __forceinline__ void conv_role(char* smem, const u16* __restrict__ are, const u16* __restrict__ aim, const u16* __restrict__ bre,
                                          const u16* __restrict__ bim, const u16* __restrict__ cre, const u16* __restrict__ cim,
                                          const u16* __restrict__ logdt, const u16* __restrict__ dskip, char* __restrict__ ws,
                                          int cw, int wave, int lane) {
  const int tid = wave * 64 + lane, n = tid & 127, half = wave >> 1;
  const int d0 = 3 * cw;
  S2V(0); S2R(14); S2ID;
  u16 raw[3][10], rdt[3], rds[3];
#pragma unroll
  for (int ch = 0; ch < 3; ++ch) {
    const int o = (d0 + ch) * NS_ + n, o2 = o ^ 64;
    rdt[ch] = logdt[d0 + ch]; rds[ch] = dskip[d0 + ch];
    raw[ch][0] = are[o]; raw[ch][1] = aim[o]; raw[ch][2] = bre[o]; raw[ch][3] = bim[o]; raw[ch][4] = cre[o]; raw[ch][5] = cim[o];
    raw[ch][6] = bre[o2]; raw[ch][7] = bim[o2]; raw[ch][8] = cre[o2]; raw[ch][9] = cim[o2];
  }
#pragma unroll
  for (int ch = 0; ch < 3; ++ch)
    glds16(ws + WS_UT + (size_t)(d0 + ch) * 4096 + tid * 16, US + ch * USTR + 480 + wave * 1024, smem);
  SCHED0;
  if (tid < 90) *(LAS u32x4*)(LAS char*)(smem + US + (tid / 30) * USTR + (tid % 30) * 16) = (u32x4)(0u);
  u32 gidx[11];
  {
    const int J = 2044 - 8 * tid;
#pragma unroll
    for (int x = -7; x <= 3; ++x) {
      int j = J + x;
      u32 bits = pk_bf16((float)j, 0.f) & 0xffffu;
      int idx = j < 256 ? j : (int)bits - 17024;
      gidx[x + 7] = KV + (j < 0 ? 740 : idx + (idx >> 3)) * 4;
    }
  }
  const int q = lane >> 4, i16 = lane & 15;
  u32 erow;
  {
    int s = wave, b = i16;
#pragma unroll
    for (int k = 0; k < 3; ++k) if (s > 0 && b < 8) { --s; b *= 2; }
    erow = TABE + (s == 0 ? b : 8 * s + b) * RS + q * 16;
  }
  SCHED0;
  float par[3][4], dsk[3], wsc[3];
#pragma unroll
  for (int ch = 0; ch < 3; ++ch) {
    const float dt = rbf(expf(bf2f(rdt[ch])));
    dsk[ch] = bf2f(rds[ch]);
    const float ar = dt * bf2f(raw[ch][0]), ai = dt * bf2f(raw[ch][1]) * 0.15915494309189535f;
    float br = dt * bf2f(raw[ch][2]), bi = dt * bf2f(raw[ch][3]), cr = bf2f(raw[ch][4]), ci = bf2f(raw[ch][5]);
    const float wr = __builtin_fmaf(cr, br, -(ci * bi)), wi = __builtin_fmaf(cr, bi, ci * br);
    br = dt * bf2f(raw[ch][6]); bi = dt * bf2f(raw[ch][7]); cr = bf2f(raw[ch][8]); ci = bf2f(raw[ch][9]);
    const float wr2 = __builtin_fmaf(cr, br, -(ci * bi)), wi2 = __builtin_fmaf(cr, bi, ci * br);
    float m = fmaxf(fmaxf(fabsf(wr), fabsf(wi)), fmaxf(fabsf(wr2), fabsf(wi2)));
    m = wave_max_nonneg(m);
    int e = m > 0.f ? __builtin_amdgcn_frexp_expf(m) : 14;
    e = e < -100 ? -100 : e;
    wsc[ch] = __builtin_ldexpf(1.f, e - 14);
    const float sc = __builtin_ldexpf(1.f, 14 - e);
    par[ch][0] = half ? ar * 16.f : ar; par[ch][1] = half ? ai * -16.f : ai;
    par[ch][2] = half ? wr * sc : 1.f; par[ch][3] = half ? -wi * sc : 0.f;
  }
  S2V(1);
  const u32 tabw = (half ? 0 : TABE) + n * 4;
  const u32 xrow = ((wave == 0 ? 0 : 8 + 8 * wave) + i16) * RS + q * 16;
  u32 kvw[4];
  {
    const int nrow = wave == 0 ? 16 : wave == 3 ? 9 : 8, r0 = wave == 0 ? 0 : 8 + 8 * wave;
#pragma unroll
    for (int i = 0; i < 4; ++i) kvw[i] = KV + (4 * q + i < nrow ? (r0 + 4 * q + i) * 18 + i16 + (i16 >> 3) : 741) * 4;
  }
  if (tid < 3) *(LAS float*)(LAS char*)(smem + KV + tid * KVS + 740 * 4) = 0.f;
  for (int ch = -1; ch < 3; ++ch) {
    f32x4 kacc = (f32x4)(0.f), kacc2 = (f32x4)(0.f);
    if (ch >= 0) {
      f16x8 ka[8], kb_[8];
#pragma unroll
      for (int kb = 0; kb < 8; ++kb) {
        ka[kb] = *(const LAS f16x8*)(LAS char*)(smem + xrow + kb * 64);
        kb_[kb] = *(const LAS f16x8*)(LAS char*)(smem + erow + kb * 64);
      }
      WAITL0; BAR;
      if (ch == 0) S2V(3);
#pragma unroll
      for (int kb = 0; kb < 8; kb += 2) {
        kacc = __builtin_amdgcn_mfma_f32_16x16x32_f16(ka[kb], kb_[kb], kacc, 0, 0, 0);
        kacc2 = __builtin_amdgcn_mfma_f32_16x16x32_f16(ka[kb + 1], kb_[kb + 1], kacc2, 0, 0, 0);
      }
      SCHED0;
    }
    if (ch < 2) {
      const int c1 = ch + 1;
      const float ar = c1 == 0 ? par[0][0] : c1 == 1 ? par[1][0] : par[2][0];
      const float ai = c1 == 0 ? par[0][1] : c1 == 1 ? par[1][1] : par[2][1];
      const f32x2 W = {c1 == 0 ? par[0][2] : c1 == 1 ? par[1][2] : par[2][2], c1 == 0 ? par[0][3] : c1 == 1 ? par[1][3] : par[2][3]};
      CP P[8];
      P[0] = mkcp(cexp_(ar, ai));
#pragma unroll
      for (int i = 1; i < 4; ++i) P[i] = mkcp(cmul(P[i - 1].v, P[i - 1]));
      P[4] = mkcp(cexp_(ar * 16.f, ai * 16.f));
#pragma unroll
      for (int i = 5; i < 8; ++i) P[i] = mkcp(cmul(P[i - 1].v, P[i - 1]));
      f32x2 X[16];
      X[0] = W; X[1] = cmul(W, P[0]); X[2] = cmul(W, P[1]); X[3] = cmul(X[1], P[1]);
#pragma unroll
      for (int k = 0; k < 4; ++k) X[4 + k] = cmul(X[k], P[2]);
#pragma unroll
      for (int k = 0; k < 8; ++k) X[8 + k] = cmul(X[k], P[3]);
#pragma unroll
      for (int a = 0; a < 16; ++a) st_h2(smem, tabw + a * RS, X[a]);
#pragma unroll
      for (int s = 1; s < 4; ++s) {
        f32x2 Y[8];
        Y[0] = cmul(W, P[s + 3]); Y[1] = cmul(Y[0], P[s]); Y[2] = cmul(Y[0], P[s + 1]); Y[3] = cmul(Y[1], P[s + 1]);
#pragma unroll
        for (int k = 0; k < 4; ++k) Y[4 + k] = cmul(Y[k], P[s + 2]);
#pragma unroll
        for (int a = 0; a < 8; ++a) st_h2(smem, tabw + (16 + 8 * (s - 1) + a) * RS, Y[a]);
      }
      st_h2(smem, tabw + 40 * RS, cmul(W, P[7]));
    }
    if (ch >= 0) {
      SCHED0;
      const float ksc = ch == 0 ? wsc[0] : ch == 1 ? wsc[1] : wsc[2];
      kacc += kacc2;
#pragma unroll
      for (int i = 0; i < 4; ++i) *(LAS float*)(LAS char*)(smem + kvw[i] + ch * KVS) = kacc[i] * ksc;
    }
    WAITL0; BAR;
    if (ch == -1) S2V(2);
    if (ch == 0) S2V(4);
  }
  S2V(5);
  {
    float V[3][11];
#pragma unroll
    for (int ch = 0; ch < 3; ++ch)
#pragma unroll
      for (int x = 0; x < 11; ++x) V[ch][x] = *(const LAS float*)(LAS char*)(smem + gidx[x] + ch * KVS);
    SCHED0;
#pragma unroll
    for (int ch = 0; ch < 3; ++ch) {
      u32 P[11];
#pragma unroll
      for (int x = 1; x < 11; ++x) P[x] = pk_bf16(V[ch][x], V[ch][x - 1]);
#pragma unroll
      for (int sg = 0; sg < 4; ++sg) {
        u32x4 c = {P[sg + 7], P[sg + 5], P[sg + 3], P[sg + 1]};
        *(LAS u32x4*)(LAS char*)(smem + ch * CPY + sg * CPS + tid * 16) = c;
      }
    }
  }
  if (tid < 24) *(LAS u32x4*)(LAS char*)(smem + (tid >> 1) * CPS + 4096 + (tid & 1) * 16) = (u32x4)(0u);
  WAITV0; WAITL0; BAR;
  S2V(6);
  auto finish = [&](const f32x4 s, const u32x2 uu, int ch, int i) {
    const float dskc = ch == 0 ? dsk[0] : ch == 1 ? dsk[1] : dsk[2];
    const int t = 256 * i + 16 * i16 + 4 * q;
    float a0 = rbf(s[0]) + rbf(lo_f(uu[0]) * dskc), a1 = rbf(s[1]) + rbf(hi_f(uu[0]) * dskc);
    float a2 = rbf(s[2]) + rbf(lo_f(uu[1]) * dskc), a3 = rbf(s[3]) + rbf(hi_f(uu[1]) * dskc);
    u32x2 pv = {pk_bf16(a0, a1), pk_bf16(a2, a3)};
    *(LAS u32x2*)(LAS char*)(smem + US + ch * USTR + 480 + t * 2) = pv;
  };
  const u32 abase0 = (i16 & 3) * CPS + (2044 - 16 + 8 * q - 4 * (i16 >> 2)) * 2;
  const u32 bbase0 = US + 480 + (16 * i16 - 16 + 8 * q) * 2;
  if (wave < 3) {
    constexpr int NTL = 8;
    f32x4 Y[NTL];
#pragma unroll
    for (int i = 0; i < NTL; ++i) Y[i] = (f32x4)(0.f);
    const u32 abase = abase0 + wave * CPY, bbase = bbase0 + wave * USTR;
    bf16x8 A0[NTL], B0[NTL], A1[NTL], B1[NTL];
    auto mm = [&](const bf16x8 (&A)[NTL], const bf16x8 (&B)[NTL], bf16x8 (&An)[NTL], bf16x8 (&Bn)[NTL], int cn, bool doit, bool more) {
      const u32 aa = abase - cn * 64, bb = bbase - cn * 64;
      u32x2 lo[NTL], hi[NTL];
      int idx = 0;
      WAITL0; SCHED0;
#pragma unroll
      for (int k = 0; k < NTL; ++k)
#pragma unroll
        for (int pp = 0; pp + k < NTL; ++pp) {
          if (doit) Y[pp + k] = __builtin_amdgcn_mfma_f32_16x16x32_bf16(A[pp], B[k], Y[pp + k], 0, 0, 0);
          if (more) {
            if (idx < NTL) Bn[idx] = *(const LAS bf16x8*)(LAS char*)(smem + bb + idx * 512);
            else if (idx < 2 * NTL) lo[idx - NTL] = *(const LAS u32x2*)(LAS char*)(smem + aa - (idx - NTL) * 512);
            else if (idx < 3 * NTL) hi[idx - 2 * NTL] = *(const LAS u32x2*)(LAS char*)(smem + aa - (idx - 2 * NTL) * 512 + 8);
          }
          ++idx;
          SCHED0;
        }
      if (more) {
#pragma unroll
        for (int pp = 0; pp < NTL; ++pp) An[pp] = __builtin_bit_cast(bf16x8, (u32x4){lo[pp][0], lo[pp][1], hi[pp][0], hi[pp][1]});
      }
    };
    mm(A1, B1, A0, B0, 0, false, true);
    for (int c = 0; c < 8; c += 2) {
      mm(A0, B0, A1, B1, c + 1, true, true);
      mm(A1, B1, A0, B0, c + 2, true, c + 2 < 8);
    }
    S2V(7);
    u32x2 uu[NTL];
#pragma unroll
    for (int i = 0; i < NTL; ++i) uu[i] = *(const LAS u32x2*)(LAS char*)(smem + US + wave * USTR + 480 + (256 * i + 16 * i16 + 4 * q) * 2);
    SCHED0;
#pragma unroll
    for (int i = 0; i < NTL; ++i) finish(Y[i], uu[i], wave, i);
    WAITL0;
    {
      const rsrc_t ra = mk_rsrc(ws + WS_AT + (size_t)(d0 + wave) * 4096, 4096);
#pragma unroll
      for (int k = 0; k < 4; ++k) {
        const u32x4 v = *(const LAS u32x4*)(LAS char*)(smem + US + wave * USTR + 480 + (k * 64 + lane) * 16);
        __builtin_amdgcn_raw_buffer_store_b128(v, ra, (k * 64 + lane) * 16, 0, AUX_SC1);
      }
    }
    S2V(8);
  }
  WAITV0; BAR;
  S2V(10); S2R(15);
  if (tid < 4) __hip_atomic_fetch_add((u32*)(ws + WS_CNT + ((d0 / 72) * 4 + tid) * 256), 1u, __ATOMIC_RELAXED, __HIP_MEMORY_SCOPE_AGENT);
}

__global__ __launch_bounds__(256, 2) void k2_gemm2_conv(const u16* __restrict__ are, const u16* __restrict__ aim,
    const u16* __restrict__ bre, const u16* __restrict__ bim, const u16* __restrict__ cre, const u16* __restrict__ cim,
    const u16* __restrict__ logdt, const u16* __restrict__ dskip, char* __restrict__ ws, u16* __restrict__ out) {
  extern __shared__ __attribute__((aligned(16))) char smem[];
  const int b = blockIdx.x;
  const int wave = __builtin_amdgcn_readfirstlane(threadIdx.x >> 6), lane = threadIdx.x & 63;
  if (b < NCONV) conv_role(smem, are, aim, bre, bim, cre, cim, logdt, dskip, ws, b, wave, lane);
  else { const int g = b - NCONV; gemm2_role(smem, ws, out, g & 7, g >> 3, wave, lane); }
}

extern "C" void kernel_launch(void* const* d_in, const int* in_sizes, int n_in, void* d_out, int out_size, void* d_ws, size_t ws_size, hipStream_t stream) {
  (void)hipFuncSetAttribute((const void*)k1_gemm1, hipFuncAttributeMaxDynamicSharedMemorySize, K1_LDS);
  (void)hipFuncSetAttribute((const void*)k2_gemm2_conv, hipFuncAttributeMaxDynamicSharedMemorySize, K2_LDS);
  k1_gemm1<<<256, 512, K1_LDS, stream>>>((const u16*)d_in[0], (const u16*)d_in[10], (const u16*)d_in[11], (const u16*)d_in[12], (const u16*)d_in[13], (char*)d_ws,
      (const u16*)d_in[1], (const u16*)d_in[2], (const u16*)d_in[3], (const u16*)d_in[4], (const u16*)d_in[5], (const u16*)d_in[6], (const u16*)d_out);
  k2_gemm2_conv<<<NCONV + 256, 256, K2_LDS, stream>>>((const u16*)d_in[1], (const u16*)d_in[2], (const u16*)d_in[3], (const u16*)d_in[4],
      (const u16*)d_in[5], (const u16*)d_in[6], (const u16*)d_in[7], (const u16*)d_in[8], (char*)d_ws, (u16*)d_out);
}
```

```cpp
#include <hip/hip_runtime.h>
typedef unsigned short u16;
typedef unsigned int u32;
typedef short s16x4 __attribute__((ext_vector_type(4)));
typedef __bf16 bf16x8 __attribute__((ext_vector_type(8)));
typedef __bf16 bf16x2 __attribute__((ext_vector_type(2)));
typedef _Float16 f16x2 __attribute__((ext_vector_type(2)));
typedef _Float16 f16x8 __attribute__((ext_vector_type(8)));
typedef float f32x2 __attribute__((ext_vector_type(2)));
typedef float f32x4 __attribute__((ext_vector_type(4)));
typedef float f32x16 __attribute__((ext_vector_type(16)));
typedef u32 u32x2 __attribute__((ext_vector_type(2)));
typedef u32 u32x4 __attribute__((ext_vector_type(4)));
#define LAS __attribute__((address_space(3)))
#define GAS __attribute__((address_space(1)))

constexpr int L_ = 2048, D_ = 576, NS_ = 128, F_ = 4608;
constexpr size_t WS_H   = 0;
constexpr size_t WS_WF  = WS_H + (size_t)32 * 144 * 4096;
constexpr size_t WS_UT  = WS_WF + (size_t)8 * 144 * 4608;
constexpr size_t WS_AT  = WS_UT + (size_t)D_ * L_ * 2;
constexpr size_t WS_G   = WS_AT + (size_t)D_ * L_ * 2;
constexpr size_t WS_CNT = WS_G + (size_t)L_ * 4;
constexpr size_t WS_DBG = 48u << 20;

__device__ __forceinline__ float bf2f(u16 v) { return __uint_as_float(((u32)v) << 16); }
__device__ __forceinline__ u32 pk_bf16(float lo, float hi) {
  f32x2 v = {lo, hi}; bf16x2 b = __builtin_convertvector(v, bf16x2); return __builtin_bit_cast(u32, b);
}
__device__ __forceinline__ float lo_f(u32 p) { return __uint_as_float(p << 16); }
__device__ __forceinline__ float hi_f(u32 p) { return __uint_as_float(p & 0xffff0000u); }
__device__ __forceinline__ void mul_halves8(u32 (&q)[8]) {
  asm("v_dot2_f32_bf16 %0, %0, %8, 0\n\tv_dot2_f32_bf16 %1, %1, %9, 0\n\tv_dot2_f32_bf16 %2, %2, %10, 0\n\tv_dot2_f32_bf16 %3, %3, %11, 0\n\t"
      "v_dot2_f32_bf16 %4, %4, %12, 0\n\tv_dot2_f32_bf16 %5, %5, %13, 0\n\tv_dot2_f32_bf16 %6, %6, %14, 0\n\tv_dot2_f32_bf16 %7, %7, %15, 0\n\ts_nop 2"
      : "+v"(q[0]), "+v"(q[1]), "+v"(q[2]), "+v"(q[3]), "+v"(q[4]), "+v"(q[5]), "+v"(q[6]), "+v"(q[7])
      : "v"(q[0] >> 16), "v"(q[1] >> 16), "v"(q[2] >> 16), "v"(q[3] >> 16), "v"(q[4] >> 16), "v"(q[5] >> 16), "v"(q[6] >> 16), "v"(q[7] >> 16));
}
__device__ __forceinline__ float wave_sum(float v) {
  v += __int_as_float(__builtin_amdgcn_update_dpp(0, __float_as_int(v), 0x111, 0xf, 0xf, true));
  v += __int_as_float(__builtin_amdgcn_update_dpp(0, __float_as_int(v), 0x112, 0xf, 0xf, true));
  v += __int_as_float(__builtin_amdgcn_update_dpp(0, __float_as_int(v), 0x114, 0xf, 0xf, true));
  v += __int_as_float(__builtin_amdgcn_update_dpp(0, __float_as_int(v), 0x118, 0xf, 0xf, true));
  const int iv = __float_as_int(v);
  return (__int_as_float(__builtin_amdgcn_readlane(iv, 15)) + __int_as_float(__builtin_amdgcn_readlane(iv, 31))) +
         (__int_as_float(__builtin_amdgcn_readlane(iv, 47)) + __int_as_float(__builtin_amdgcn_readlane(iv, 63)));
}
__device__ __forceinline__ float rbf(float x) { return lo_f(pk_bf16(x, 0.f)); }

__device__ __forceinline__ void glds16(const void* g, u32 lds_byte, char* smem) {
  __builtin_amdgcn_global_load_lds((const GAS void*)g, (LAS void*)(smem + lds_byte), 16, 0, 0);
}
typedef __amdgpu_buffer_rsrc_t rsrc_t;
__device__ __forceinline__ rsrc_t mk_rsrc(const void* p, u32 bytes) {
  return __builtin_amdgcn_make_buffer_rsrc((void*)p, 0, bytes, 0x00020000);
}
__device__ __forceinline__ void blds16(rsrc_t r, u32 voff, u32 soff, u32 lds_byte, char* smem) {
  __builtin_amdgcn_raw_ptr_buffer_load_lds(r, (LAS void*)(smem + lds_byte), 16, voff, soff, 0, 0);
}
__device__ __forceinline__ s16x4 tr_read(char* smem, u32 off) {
  return __builtin_amdgcn_ds_read_tr16_b64_v4i16((LAS s16x4*)(smem + off));
}
#define WAITV0 asm volatile("s_waitcnt vmcnt(0)" ::: "memory")
#define WAITL0 asm volatile("s_waitcnt lgkmcnt(0)" ::: "memory")
#define BAR __builtin_amdgcn_s_barrier()
#define SCHED0 __builtin_amdgcn_sched_barrier(0)

#define DMA_EVERY 2
#define DMA_N0 4
#define DMA_N1 4
constexpr int X0_ = 0, XST = 32768, W0_ = 65536, WST = 36992, UOFF = 18432 + 128;
constexpr int WDN = X0_ + XST;
constexpr int UTS = W0_ + 2 * WST;
constexpr int GWL = UTS + 9216;
constexpr int K1_LDS = GWL + 1152;

template <int NT>
struct Frag { bf16x8 x[2]; bf16x8 w[NT]; };

template <int NT>
__device__ __forceinline__ void k1_load_frags(Frag<NT>& f, char* smem, u32 xaddr, u32 waddr, int stage, int s) {
#pragma unroll
  for (int xi = 0; xi < 2; ++xi)
    f.x[xi] = *(const LAS bf16x8*)(LAS char*)(smem + xaddr + stage * XST + xi * 4096);
#pragma unroll
  for (int T = 0; T < NT; ++T) {
    s16x4 a = tr_read(smem, waddr + stage * WST + (16 * s) * 288 + 32 * T);
    s16x4 b = tr_read(smem, waddr + stage * WST + (16 * s + 4) * 288 + 32 * T);
    typedef short s16x8 __attribute__((ext_vector_type(8)));
    s16x8 c = __builtin_shufflevector(a, b, 0, 1, 2, 3, 4, 5, 6, 7);
    f.w[T] = __builtin_bit_cast(bf16x8, c);
  }
}
template <int NT>
__device__ __forceinline__ void k1_mma(f32x16 (&acc)[NT][2], const Frag<NT>& f) {
#pragma unroll
  for (int T = 0; T < NT; ++T)
#pragma unroll
    for (int xi = 0; xi < 2; ++xi)
      acc[T][xi] = __builtin_amdgcn_mfma_f32_32x32x16_bf16(f.w[T], f.x[xi], acc[T][xi], 0, 0, 0);
}
template <int NT, int NDMA>
__device__ __forceinline__ void k1_interleave() {
#pragma unroll
  for (int i = 0; i < NT * 2; ++i) {
    __builtin_amdgcn_sched_group_barrier(0x8, 1, 0);
    if (i < 2) __builtin_amdgcn_sched_group_barrier(0x100, 2, 0); else __builtin_amdgcn_sched_group_barrier(0x100, 1, 0);
    if (i < NDMA) __builtin_amdgcn_sched_group_barrier(0x10, 1, 0);
  }
}

template <int NT, int WN>
__device__ __forceinline__ void k1_body(char* smem, const u16* __restrict__ x, const u16* __restrict__ wg, const u16* __restrict__ wu,
                                        const u16* __restrict__ wd, const u16* __restrict__ gw, char* __restrict__ ws, int ib, int jb, int wave, int lane,
                                        const u16* p0, const u16* p1, const u16* p2, const u16* p3, const u16* p4, const u16* p5, const u16* p6, const u16* p7, const u16* outp) {
  constexpr int NW = (WN == 0) ? 4 : 5;
  const int wm = wave & 3;
  u32 xoff[4], woff[NW];
#pragma unroll
  for (int ii = 0; ii < 4; ++ii) {
    int rr = lane >> 3, row = 32 * wave + 8 * ii + rr;
    int c = (lane & 7) ^ ((row >> 1) & 7);
    xoff[ii] = row * 1152 + c * 16;
  }
  const int wi0 = (WN == 0) ? 4 * wave : 16 + 5 * (wave - 4);
#pragma unroll
  for (int k = 0; k < NW; ++k) {
    int wi = wi0 + k; int gi = wi >= 18 ? wi - 18 : wi;
    int ci = 64 * gi + lane, row = ci / 18, w18 = ci - row * 18;
    int colb = (w18 < 16) ? (256 * jb + 16 * w18) : (8192 + 32 * jb + 16 * (w18 - 16));
    woff[k] = row * 9216 + colb;
  }
  const rsrc_t rx = mk_rsrc((const char*)x + (size_t)ib * 256 * 1152, 256 * 1152);
  const rsrc_t rg = mk_rsrc(wg, D_ * F_ * 2), ru = mk_rsrc(wu, D_ * F_ * 2);
  const int r32 = lane & 31, hh = lane >> 5;
  u32 xa[4];
#pragma unroll
  for (int s = 0; s < 4; ++s) xa[s] = X0_ + (64 * wm + r32) * 128 + (((2 * s + hh) ^ ((r32 >> 1) & 7)) * 16);
  const int g4 = lane >> 4;
  const u32 wa = W0_ + ((g4 & 1) ? UOFF : 0) + (8 * (g4 >> 1) + ((lane & 15) >> 2)) * 288 + (lane & 3) * 8 + (WN ? 5 * 32 : 0);

  f32x16 acc[NT][2];
#pragma unroll
  for (int T = 0; T < NT; ++T) { acc[T][0] = (f32x16)(0.f); acc[T][1] = (f32x16)(0.f); }
  Frag<NT> f0, f1;
  const int lane_ = lane;

  auto dma1 = [&](int t, int stage, int d) {
    if (d < 4) blds16(rx, xoff[d], t * 128, X0_ + stage * XST + (32 * wave + 8 * d) * 128, smem);
    else {
      const int k = d - 4; int wi = wi0 + k; bool up = wi >= 18; int gi = up ? wi - 18 : wi;
      blds16(up ? ru : rg, woff[k], t * 64 * 9216, W0_ + stage * WST + (up ? UOFF : 0) + gi * 1024, smem);
    }
  };
  float gs0 = 0.f, gs1 = 0.f;
  u32 ux0 = 0, ux1 = 0, ugw = 0;
  const int urow = 8 * jb + 2 * (wave - 4);
  const u32 uaddr = X0_ + urow * 128 + (((lane >> 3) ^ ((urow >> 1) & 7)) << 4) + (lane & 7) * 2;
  auto sub = [&](Frag<NT>& cur, Frag<NT>& nxt, bool do_mma, int stage, int s, int t1, int d0, int nd) {
    typedef short s16x8 __attribute__((ext_vector_type(8)));
    s16x4 ta[NT], tb[NT];
    int di = 0;
#pragma unroll
    for (int i = 0; i < 2 * NT; ++i) {
      if (do_mma) acc[i >> 1][i & 1] = __builtin_amdgcn_mfma_f32_32x32x16_bf16(cur.w[i >> 1], cur.x[i & 1], acc[i >> 1][i & 1], 0, 0, 0);
      if (i == 0) {
        nxt.x[0] = *(const LAS bf16x8*)(LAS char*)(smem + xa[s] + stage * XST);
        nxt.x[1] = *(const LAS bf16x8*)(LAS char*)(smem + xa[s] + stage * XST + 4096);
      } else if (i == 1) {
        ta[0] = tr_read(smem, wa + stage * WST + (16 * s) * 288);
        tb[0] = tr_read(smem, wa + stage * WST + (16 * s + 4) * 288);
      } else {
        const int T = i >> 1;
        if ((i & 1) == 0) ta[T] = tr_read(smem, wa + stage * WST + (16 * s) * 288 + 32 * T);
        else              tb[T] = tr_read(smem, wa + stage * WST + (16 * s + 4) * 288 + 32 * T);
      }
      if (WN == 1) {
        if (s == 0 && i == 7) {
          ux0 = *(const LAS u16*)(LAS char*)(smem + uaddr + stage * XST);
          ux1 = *(const LAS u16*)(LAS char*)(smem + uaddr + stage * XST + 128);
          ugw = *(const LAS u16*)(LAS char*)(smem + GWL + ((t1 - 1) * 64 + lane) * 2);
        }
        if (s == 1 && i == 4) gs0 += rbf(__uint_as_float(ux0 << 16) * __uint_as_float(ugw << 16));
        if (s == 1 && i == 5) gs1 += rbf(__uint_as_float(ux1 << 16) * __uint_as_float(ugw << 16));
        if (s == 1 && i == 6) *(LAS u32*)(LAS char*)(smem + UTS + ((t1 - 1) * 64 + lane) * 16 + (wave - 4) * 4) = ux0 | (ux1 << 16);
      }
      if (di < nd && (i % DMA_EVERY) == (WN ? 1 : 2) % DMA_EVERY) { dma1(t1, stage ^ 1, d0 + di); ++di; }
      SCHED0;
    }
#pragma unroll
    for (int k = 0; k < 9; ++k) if (di < nd) { dma1(t1, stage ^ 1, d0 + di); ++di; }
#pragma unroll
    for (int T = 0; T < NT; ++T) nxt.w[T] = __builtin_bit_cast(bf16x8, __builtin_shufflevector(ta[T], tb[T], 0, 1, 2, 3, 4, 5, 6, 7));
    SCHED0;
  };
  auto wrelay = [&](int T) {
    int lane = lane_; asm volatile("" : "+v"(lane));
    const int g4 = lane >> 4, i16 = lane & 15, hh2 = g4 & 1;
    const int G = 9 * jb + T, qq = 2 * (G & 1) + hh2;
    char* dst0 = ws + WS_WF + (size_t)(ib * 144 + (G >> 1)) * 4608;
#pragma unroll
    for (int r = 0; r < 3; ++r) {
      const int item = 2 * r + (g4 >> 1), m = item < 5 ? item : 4;
      const u32 la = WDN + (16 * T + 4 * hh2 + (i16 >> 2)) * 144 + m * 32 + (i16 & 3) * 8;
      s16x4 a = tr_read(smem, la), b = tr_read(smem, la + 8 * 144);
      typedef short s16x8 __attribute__((ext_vector_type(8)));
      const u32x4 v = __builtin_bit_cast(u32x4, __builtin_shufflevector(a, b, 0, 1, 2, 3, 4, 5, 6, 7));
      char* dst = dst0 + (m < 4 ? m * 1024 + (qq * 16 + i16) * 16 : 4096 + (qq * 8 + i16) * 16);
      if (item < 5 && (m < 4 || i16 < 8)) *(u32x4*)dst = v;
    }
  };
  u32 pfd = 0;
  u32 pfo;
  if (WN == 0) { const int l2 = (ib & 3) * 48 + wave * 12 + lane, row = l2 / 3, c = l2 - 3 * row; pfo = row * 9216 + (c < 2 ? 256 * jb + 128 * c : 8192 + 32 * jb); }
  else pfo = ((jb & 3) * 64 + (wave - 4) * 16 + lane) * 1152;
  const rsrc_t rpf = WN == 0 ? mk_rsrc(ib >= 4 ? wu : wg, D_ * F_ * 2) : rx;
  auto pf = [&](int tp) {
    if (tp <= 8) {
      const u32 so = WN == 0 ? tp * 64 * 9216 : tp * 128;
      if (lane < (WN == 0 ? 12 : 16)) asm volatile("buffer_load_dword %0, %1, %2, %3 offen" : "+v"(pfd) : "v"(pfo), "s"(rpf), "s"(so) : "memory");
    }
  };
  auto wdn_dma = [&](int T) {
    int lane = lane_; asm volatile("" : "+v"(lane));
    const rsrc_t rd = mk_rsrc((const char*)wd + ib * 144, F_ * D_ * 2 - ib * 144);
    const int f0 = T < 8 ? 128 * jb + 16 * T : 4096 + 16 * jb;
#pragma unroll
    for (int r = 0; r < 3; ++r) {
      const int ci = 64 * r + lane;
      if (ci < 144) {
        const int row = ci / 9, cc = ci - 9 * row;
        blds16(rd, (f0 + row) * 1152 + cc * 16, 0, WDN + T * 2304 + r * 1024, smem);
      }
    }
  };
  auto step = [&](int t, int stage, bool first, bool last) {
    {
      int ne = 0;
      ne += first ? 2 : (t - 1 <= 5 ? 1 : 0);
      ne += first ? 1 : 0;
      if (ne == 0) WAITV0;
      else if (ne == 1) asm volatile("s_waitcnt vmcnt(1)" ::: "memory");
      else if (ne == 2) asm volatile("s_waitcnt vmcnt(2)" ::: "memory");
      else if (ne == 3) asm volatile("s_waitcnt vmcnt(3)" ::: "memory");
      else asm volatile("s_waitcnt vmcnt(4)" ::: "memory");
    }
    WAITL0; BAR; SCHED0;
    if (last) { wdn_dma(wave); if (wave == 7) wdn_dma(8); SCHED0; }
    constexpr int ND = 4 + NW;
    constexpr int N0 = DMA_N0, N1 = (ND - N0 < DMA_N1) ? ND - N0 : DMA_N1, N2 = ND - N0 - N1;
    sub(f1, f0, !first, stage, 0, t + 1, 0, last ? 0 : N0);
    sub(f0, f1, true, stage, 1, t + 1, N0, last ? 0 : N1);
    sub(f1, f0, true, stage, 2, t + 1, N0 + N1, last ? 0 : N2);
    SCHED0;
    if (t <= 5) pf(t + 3);
    SCHED0;
    sub(f0, f1, true, stage, 3, t + 1, 0, 0);
  };
  if (wave == 4) {
    const rsrc_t rgw = mk_rsrc(gw, D_ * 2);
    blds16(rgw, lane * 16, 0, GWL, smem);
    if (lane < 8) blds16(rgw, 1024 + lane * 16, 0, GWL + 1024, smem);
  }
#pragma unroll
  for (int p = 0; p < 4 + NW; ++p) dma1(0, 0, p);
pf(1); pf(2);
  {
    const int g = (int)(blockIdx.x >> 3) * 16 + wave * 2 + (lane & 1);
    const char* pa = lane < 2 ? ws + (size_t)min(g, (int)(WS_CNT >> 16)) * 65536 : (const char*)outp + (size_t)min(g, 35) * 65536;
    if (lane < 2 || (lane < 4 && g < 36)) asm volatile("global_load_dword %0, %1, off" : "+v"(pfd) : "v"(pa) : "memory");
  }
  step(0, 0, true, false);
  for (int t = 1; t < 7; t += 2) { step(t, 1, false, false); step(t + 1, 0, false, false); }
  step(7, 1, false, false);
  step(8, 0, false, true);
  k1_mma<NT>(acc, f1);
  SCHED0;
  if (WN == 0 && wave == 0) {
    int ln = lane_; asm volatile("" : "+v"(ln));
    const int a = min(ln / 6, 5), c = ln % 6;
    const u16* pa = a == 0 ? p0 : a == 1 ? p1 : a == 2 ? p2 : a == 3 ? p3 : a == 4 ? p4 : p5;
    pa = ln < 36 ? pa + blockIdx.x * 384 + c * 64 : (ln == 36 ? p6 : p7) + (blockIdx.x * 3 & ~1);
    if (blockIdx.x < 192 && ln < 38) asm volatile("global_load_dword %0, %1, off" : "+v"(pfd) : "v"(pa) : "memory");
  }
  if (WN == 1) {
    const float t0 = wave_sum(gs0), t1 = wave_sum(gs1);
    const float v = rbf(lane == 0 ? t0 : t1);
    if (lane < 2) ((float*)(ws + WS_G))[256 * ib + urow + lane] = rbf(1.f / (1.f + expf(-v)));
    if (blockIdx.x == 0 && wave == 4 && lane < 32) *(u32*)(ws + WS_CNT + lane * 256) = 0u;
  }

  char* hb = ws + WS_H + (size_t)(4 * ib + wm) * 144 * 4096 + ((r32 >> 4) * 1024 + (hh * 16 + (r32 & 15)) * 16);
#pragma unroll
  for (int T = 0; T < NT; ++T) {
    const int G = 9 * jb + T + (WN ? 5 : 0);
#pragma unroll
    for (int xi = 0; xi < 2; ++xi) {
      u32x4 o;
      u32 qq[8];
#pragma unroll
      for (int i = 0; i < 8; ++i) qq[i] = pk_bf16(acc[T][xi][i], acc[T][xi][8 + i]);
      mul_halves8(qq);
#pragma unroll
      for (int i = 0; i < 4; ++i) o[i] = pk_bf16(__uint_as_float(qq[2 * i]), __uint_as_float(qq[2 * i + 1]));
      *(u32x4*)(hb + (size_t)(G >> 1) * 4096 + xi * 2048 + (G & 1) * 512) = o;
    }
  }
  WAITL0; BAR; SCHED0;
  {
    const int tid = wave * 64 + lane;
    char* ut = ws + WS_UT + (256 * ib + 8 * jb) * 2;
    *(u32x4*)(ut + (size_t)tid * 4096) = *(const LAS u32x4*)(LAS char*)(smem + UTS + tid * 16);
    if (tid < 64) *(u32x4*)(ut + (size_t)(512 + tid) * 4096) = *(const LAS u32x4*)(LAS char*)(smem + UTS + (512 + tid) * 16);
  }
  WAITV0; SCHED0;
  wrelay(wave);
  if (wave == 7) wrelay(8);
  WAITV0;
  asm volatile("" :: "v"(pfd));
}

__global__ __launch_bounds__(512, 2) void k1_gemm1(const u16* __restrict__ x, const u16* __restrict__ wg, const u16* __restrict__ wu,
                                                   const u16* __restrict__ wd, const u16* __restrict__ gw, char* __restrict__ ws,
                                                   const u16* p0, const u16* p1, const u16* p2, const u16* p3, const u16* p4, const u16* p5, const u16* p6, const u16* p7, const u16* outp) {
  extern __shared__ __attribute__((aligned(16))) char smem[];
  const int b = blockIdx.x, idx = b >> 3;
  const int ib = idx & 7, jb = 4 * (b & 7) + (idx >> 3);
  const int wave = __builtin_amdgcn_readfirstlane(threadIdx.x >> 6), lane = threadIdx.x & 63;
  if (wave < 4) k1_body<5, 0>(smem, x, wg, wu, wd, gw, ws, ib, jb, wave, lane, p0, p1, p2, p3, p4, p5, p6, p7, outp);
  else          k1_body<4, 1>(smem, x, wg, wu, wd, gw, ws, ib, jb, wave, lane, p0, p1, p2, p3, p4, p5, p6, p7, outp);
}

#define S2V(i)
#define S2R(i)
#define S2ID
constexpr int K2_LDS = 72672;
constexpr int NCONV = 192;
constexpr u32 AUX_SC1 = 16;

__device__ __forceinline__ u32x4 bload16(rsrc_t r, u32 voff, u32 soff) {
  return __builtin_bit_cast(u32x4, __builtin_amdgcn_raw_buffer_load_b128(r, voff, soff, 0));
}
__device__ __forceinline__ int gbase(int G) {
  int jb = G / 9, T = G - 9 * jb;
  return T < 8 ? 128 * jb + 16 * T : 4096 + 16 * jb;
}

#define G2D 2
__device__ __forceinline__ void gemm2_role(char* smem, char* __restrict__ ws, u16* __restrict__ out,
                                           int ib, int idx, int wave, int lane) {
  const int rowsub = idx & 3, cb = idx >> 2, rb64 = 4 * ib + rowsub;
  const rsrc_t rh = mk_rsrc(ws + WS_H + (size_t)rb64 * 144 * 4096, 144 * 4096);
  const rsrc_t rw = mk_rsrc(ws + WS_WF + (size_t)cb * 144 * 4608, 144 * 4608);
  const int q = lane >> 4, i16 = lane & 15;
  f32x4 acc[5][4];
#pragma unroll
  for (int a = 0; a < 5; ++a)
#pragma unroll
    for (int b = 0; b < 4; ++b) acc[a][b] = (f32x4)(0.f);
  u32x4 hb[G2D][4], wb[G2D][5];
  const u32 hv = lane * 16, hv4 = 4096 + (q * 8 + (i16 & 7)) * 16;
  auto ldh = [&](int j, int r, int ks) { hb[j][r] = bload16(rh, hv + r * 1024, ks * 4096); };
  auto ldw = [&](int j, int m, int ks) { wb[j][m] = m < 4 ? bload16(rw, hv + m * 1024, ks * 4608) : bload16(rw, hv4, ks * 4608); };
  auto ldall = [&](int j, int ks) {
#pragma unroll
    for (int r = 0; r < 3; ++r) ldh(j, r, ks);
#pragma unroll
    for (int m = 0; m < 5; ++m) ldw(j, m, ks);
    ldh(j, 3, ks);
    SCHED0;
  };
  auto kstep = [&](int j, int ksn, bool more) {
#pragma unroll
    for (int r = 0; r < 4; ++r) {
#pragma unroll
      for (int m = 0; m < 5; ++m) {
        acc[m][r] = __builtin_amdgcn_mfma_f32_16x16x32_bf16(__builtin_bit_cast(bf16x8, wb[j][m]), __builtin_bit_cast(bf16x8, hb[j][r]), acc[m][r], 0, 0, 0);
        if (r == 3 && more) { SCHED0; ldw(j, m, ksn); SCHED0; }
      }
      SCHED0;
      if (more) ldh(j, r, ksn);
      SCHED0;
    }
  };
  S2V(0); S2R(14); S2ID;
#pragma unroll
  for (int j = 0; j < G2D; ++j) ldall(j, wave + 4 * j);
  SCHED0;
  constexpr int NIT = 36 / G2D;
  const u32* cnt = (const u32*)(ws + WS_CNT + (cb * 4 + rowsub) * 256);
  u32 early = 0;
  for (int it = 0; it < NIT - 1; ++it) {
    if (it == NIT - 2) early = __hip_atomic_load(cnt, __ATOMIC_RELAXED, __HIP_MEMORY_SCOPE_AGENT);
#pragma unroll
    for (int j = 0; j < G2D; ++j) kstep(j, wave + 4 * (G2D * (it + 1) + j), true);
    if (it == 0) S2V(1);
  }
  S2V(2);
  const int tid = wave * 64 + lane;
  u32x4 av[3];
  const float gv = ((const float*)(ws + WS_G))[rb64 * 64 + 16 * wave + i16];
  const rsrc_t ra = mk_rsrc(ws + WS_AT + (size_t)cb * 72 * 4096 + rb64 * 128, 72 * 4096);
  auto lda = [&]() {
#pragma unroll
    for (int k = 0; k < 3; ++k) {
      const int ci = min(tid + 256 * k, 575);
      av[k] = __builtin_bit_cast(u32x4, __builtin_amdgcn_raw_buffer_load_b128(ra, (ci >> 3) * 4096 + (ci & 7) * 16, 0, AUX_SC1));
    }
  };
  const bool ready = __builtin_amdgcn_readfirstlane(early) >= NCONV / 8;
  if (ready) lda();
  SCHED0;
  S2V(3);
#pragma unroll
  for (int j = 0; j < G2D; ++j) kstep(j, 0, false);
  S2V(4);

#pragma unroll
  for (int r = 0; r < 4; ++r) {
    if (r != wave) {
      const int slot = (wave - r - 1) & 3;
#pragma unroll
      for (int dt = 0; dt < 5; ++dt)
        *(LAS f32x4*)(LAS char*)(smem + ((r * 3 + slot) * 5 + dt) * 1024 + lane * 16) = acc[dt][r];
    }
  }
  f32x4 own[5];
#pragma unroll
  for (int dt = 0; dt < 5; ++dt) own[dt] = wave == 0 ? acc[dt][0] : wave == 1 ? acc[dt][1] : wave == 2 ? acc[dt][2] : acc[dt][3];
  if (lane == 0) *(LAS u32*)(LAS char*)(smem + 71808 + wave * 4) = ready ? 1u : 0u;
  WAITL0; BAR; asm volatile("" ::: "memory");
  const u32x4 rdy4 = *(const LAS u32x4*)(LAS char*)(smem + 71808);
  const bool allready = __builtin_amdgcn_readfirstlane(rdy4[0] & rdy4[1] & rdy4[2] & rdy4[3]) != 0;
  const float omg = rbf(1.f - gv);
#pragma unroll
  for (int dt = 0; dt < 5; ++dt) {
#pragma unroll
    for (int sl = 0; sl < 3; ++sl) own[dt] += *(const LAS f32x4*)(LAS char*)(smem + ((wave * 3 + sl) * 5 + dt) * 1024 + lane * 16);
#pragma unroll
    for (int i = 0; i < 4; ++i) own[dt][i] = rbf(omg * rbf(own[dt][i]));
  }
  S2V(5);
  if (!allready) {
    if (wave == 0) {
      for (int it = 0; it < (1 << 22); ++it) {
        if (__hip_atomic_load(cnt, __ATOMIC_RELAXED, __HIP_MEMORY_SCOPE_AGENT) >= NCONV / 8) break;
        __builtin_amdgcn_s_sleep(1);
      }
    }
    BAR; asm volatile("" ::: "memory");
    if (!ready) lda();
  }
#pragma unroll
  for (int k = 0; k < 3; ++k) {
    const int ci = tid + 256 * k;
    if (ci < 576) *(LAS u32x4*)(LAS char*)(smem + 61440 + (ci >> 3) * 144 + (ci & 7) * 16) = av[k];
  }
  WAITL0; BAR; asm volatile("" ::: "memory");
  S2V(8);
  u16* orow = out + (size_t)(rb64 * 64 + 16 * wave + i16) * D_ + cb * 72 + 4 * q;
#pragma unroll
  for (int dt = 0; dt < 5; ++dt) {
    if (dt < 4 || q < 2) {
      float o[4];
#pragma unroll
      for (int i = 0; i < 4; ++i) {
        float av = bf2f(*(const LAS u16*)(LAS char*)(smem + 61440 + (16 * dt + 4 * q + i) * 144 + (16 * wave + i16) * 2));
        o[i] = rbf(gv * av) + own[dt][i];
      }
      u32x2 pv = {pk_bf16(o[0], o[1]), pk_bf16(o[2], o[3])};
      *(u32x2*)(orow + 16 * dt) = pv;
    }
  }
  S2V(6);
  S2V(7); S2R(15);
}

constexpr int RS = 528, TABE = 41 * RS, CPS = 4160, CPY = 4 * CPS, KV = 3 * CPY, KVS = 2976, US = KV + 3 * KVS, USTR = 4608;
struct CP { f32x2 v, r; };
__device__ __forceinline__ CP mkcp(f32x2 p) { CP c; c.v = p; c.r = (f32x2){-p.y, p.x}; return c; }
__device__ __forceinline__ f32x2 cmul(f32x2 a, const CP& p) {
  return __builtin_elementwise_fma(p.r, (f32x2){a.y, a.y}, p.v * (f32x2){a.x, a.x});
}
__device__ __forceinline__ f32x2 cexp_(float ar, float air) {
  float e = __builtin_amdgcn_exp2f(ar * 1.44269504088896341f);
  float fr = __builtin_amdgcn_fractf(air);
  return (f32x2){e * __builtin_amdgcn_cosf(fr), e * __builtin_amdgcn_sinf(fr)};
}
__device__ __forceinline__ float wave_max_nonneg(float m) {
  int v = __float_as_int(m);
  v = max(v, __builtin_amdgcn_update_dpp(0, v, 0x111, 0xf, 0xf, true));
  v = max(v, __builtin_amdgcn_update_dpp(0, v, 0x112, 0xf, 0xf, true));
  v = max(v, __builtin_amdgcn_update_dpp(0, v, 0x114, 0xf, 0xf, true));
  v = max(v, __builtin_amdgcn_update_dpp(0, v, 0x118, 0xf, 0xf, true));
  const int a = __builtin_amdgcn_readlane(v, 15), b = __builtin_amdgcn_readlane(v, 31);
  const int c = __builtin_amdgcn_readlane(v, 47), d = __builtin_amdgcn_readlane(v, 63);
  return __int_as_float(max(max(a, b), max(c, d)));
}
__device__ __forceinline__ void st_h2(char* smem, u32 off, f32x2 v) {
  *(LAS f16x2*)(LAS char*)(smem + off) = __builtin_convertvector(v, f16x2);
}

__device__ __forceinline__ void conv_role(char* smem, const u16* __restrict__ are, const u16* __restrict__ aim, const u16* __restrict__ bre,
                                          const u16* __restrict__ bim, const u16* __restrict__ cre, const u16* __restrict__ cim,
                                          const u16* __restrict__ logdt, const u16* __restrict__ dskip, char* __restrict__ ws,
                                          int cw, int wave, int lane) {
  const int tid = wave * 64 + lane, n = tid & 127, half = wave >> 1;
  const int d0 = 3 * cw;
  S2V(0); S2R(14); S2ID;
  u16 raw[3][10], rdt[3], rds[3];
#pragma unroll
  for (int ch = 0; ch < 3; ++ch) {
    const int o = (d0 + ch) * NS_ + n, o2 = o ^ 64;
    rdt[ch] = logdt[d0 + ch]; rds[ch] = dskip[d0 + ch];
    raw[ch][0] = are[o]; raw[ch][1] = aim[o]; raw[ch][2] = bre[o]; raw[ch][3] = bim[o]; raw[ch][4] = cre[o]; raw[ch][5] = cim[o];
    raw[ch][6] = bre[o2]; raw[ch][7] = bim[o2]; raw[ch][8] = cre[o2]; raw[ch][9] = cim[o2];
  }
#pragma unroll
  for (int ch = 0; ch < 3; ++ch)
    glds16(ws + WS_UT + (size_t)(d0 + ch) * 4096 + tid * 16, US + ch * USTR + 480 + wave * 1024, smem);
  SCHED0;
  if (tid < 90) *(LAS u32x4*)(LAS char*)(smem + US + (tid / 30) * USTR + (tid % 30) * 16) = (u32x4)(0u);
  u32 gidx[11];
  {
    const int J = 2044 - 8 * tid;
#pragma unroll
    for (int x = -7; x <= 3; ++x) {
      int j = J + x;
      u32 bits = pk_bf16((float)j, 0.f) & 0xffffu;
      int idx = j < 256 ? j : (int)bits - 17024;
      gidx[x + 7] = KV + (j < 0 ? 740 : idx + (idx >> 3)) * 4;
    }
  }
  const int q = lane >> 4, i16 = lane & 15;
  u32 erow;
  {
    int s = wave, b = i16;
#pragma unroll
    for (int k = 0; k < 3; ++k) if (s > 0 && b < 8) { --s; b *= 2; }
    erow = TABE + (s == 0 ? b : 8 * s + b) * RS + q * 16;
  }
  SCHED0;
  float par[3][4], dsk[3], wsc[3];
#pragma unroll
  for (int ch = 0; ch < 3; ++ch) {
    const float dt = rbf(expf(bf2f(rdt[ch])));
    dsk[ch] = bf2f(rds[ch]);
    const float ar = dt * bf2f(raw[ch][0]), ai = dt * bf2f(raw[ch][1]) * 0.15915494309189535f;
    float br = dt * bf2f(raw[ch][2]), bi = dt * bf2f(raw[ch][3]), cr = bf2f(raw[ch][4]), ci = bf2f(raw[ch][5]);
    const float wr = __builtin_fmaf(cr, br, -(ci * bi)), wi = __builtin_fmaf(cr, bi, ci * br);
    br = dt * bf2f(raw[ch][6]); bi = dt * bf2f(raw[ch][7]); cr = bf2f(raw[ch][8]); ci = bf2f(raw[ch][9]);
    const float wr2 = __builtin_fmaf(cr, br, -(ci * bi)), wi2 = __builtin_fmaf(cr, bi, ci * br);
    float m = fmaxf(fmaxf(fabsf(wr), fabsf(wi)), fmaxf(fabsf(wr2), fabsf(wi2)));
    m = wave_max_nonneg(m);
    int e = m > 0.f ? __builtin_amdgcn_frexp_expf(m) : 14;
    e = e < -100 ? -100 : e;
    wsc[ch] = __builtin_ldexpf(1.f, e - 14);
    const float sc = __builtin_ldexpf(1.f, 14 - e);
    par[ch][0] = half ? ar * 16.f : ar; par[ch][1] = half ? ai * -16.f : ai;
    par[ch][2] = half ? wr * sc : 1.f; par[ch][3] = half ? -wi * sc : 0.f;
  }
  S2V(1);
  const u32 tabw = (half ? 0 : TABE) + n * 4;
  const u32 xrow = ((wave == 0 ? 0 : 8 + 8 * wave) + i16) * RS + q * 16;
  u32 kvw[4];
  {
    const int nrow = wave == 0 ? 16 : wave == 3 ? 9 : 8, r0 = wave == 0 ? 0 : 8 + 8 * wave;
#pragma unroll
    for (int i = 0; i < 4; ++i) kvw[i] = KV + (4 * q + i < nrow ? (r0 + 4 * q + i) * 18 + i16 + (i16 >> 3) : 741) * 4;
  }
  if (tid < 3) *(LAS float*)(LAS char*)(smem + KV + tid * KVS + 740 * 4) = 0.f;
  for (int ch = -1; ch < 3; ++ch) {
    f32x4 kacc = (f32x4)(0.f), kacc2 = (f32x4)(0.f);
    if (ch >= 0) {
      f16x8 ka[8], kb_[8];
#pragma unroll
      for (int kb = 0; kb < 8; ++kb) {
        ka[kb] = *(const LAS f16x8*)(LAS char*)(smem + xrow + kb * 64);
        kb_[kb] = *(const LAS f16x8*)(LAS char*)(smem + erow + kb * 64);
      }
      WAITL0; BAR;
      if (ch == 0) S2V(3);
#pragma unroll
      for (int kb = 0; kb < 8; kb += 2) {
        kacc = __builtin_amdgcn_mfma_f32_16x16x32_f16(ka[kb], kb_[kb], kacc, 0, 0, 0);
        kacc2 = __builtin_amdgcn_mfma_f32_16x16x32_f16(ka[kb + 1], kb_[kb + 1], kacc2, 0, 0, 0);
      }
      SCHED0;
    }
    if (ch < 2) {
      const int c1 = ch + 1;
      const float ar = c1 == 0 ? par[0][0] : c1 == 1 ? par[1][0] : par[2][0];
      const float ai = c1 == 0 ? par[0][1] : c1 == 1 ? par[1][1] : par[2][1];
      const f32x2 W = {c1 == 0 ? par[0][2] : c1 == 1 ? par[1][2] : par[2][2], c1 == 0 ? par[0][3] : c1 == 1 ? par[1][3] : par[2][3]};
      CP P[8];
      P[0] = mkcp(cexp_(ar, ai));
#pragma unroll
      for (int i = 1; i < 4; ++i) P[i] = mkcp(cmul(P[i - 1].v, P[i - 1]));
      P[4] = mkcp(cexp_(ar * 16.f, ai * 16.f));
#pragma unroll
      for (int i = 5; i < 8; ++i) P[i] = mkcp(cmul(P[i - 1].v, P[i - 1]));
      f32x2 X[16];
      X[0] = W; X[1] = cmul(W, P[0]); X[2] = cmul(W, P[1]); X[3] = cmul(X[1], P[1]);
#pragma unroll
      for (int k = 0; k < 4; ++k) X[4 + k] = cmul(X[k], P[2]);
#pragma unroll
      for (int k = 0; k < 8; ++k) X[8 + k] = cmul(X[k], P[3]);
#pragma unroll
      for (int a = 0; a < 16; ++a) st_h2(smem, tabw + a * RS, X[a]);
#pragma unroll
      for (int s = 1; s < 4; ++s) {
        f32x2 Y[8];
        Y[0] = cmul(W, P[s + 3]); Y[1] = cmul(Y[0], P[s]); Y[2] = cmul(Y[0], P[s + 1]); Y[3] = cmul(Y[1], P[s + 1]);
#pragma unroll
        for (int k = 0; k < 4; ++k) Y[4 + k] = cmul(Y[k], P[s + 2]);
#pragma unroll
        for (int a = 0; a < 8; ++a) st_h2(smem, tabw + (16 + 8 * (s - 1) + a) * RS, Y[a]);
      }
      st_h2(smem, tabw + 40 * RS, cmul(W, P[7]));
    }
    if (ch >= 0) {
      SCHED0;
      const float ksc = ch == 0 ? wsc[0] : ch == 1 ? wsc[1] : wsc[2];
      kacc += kacc2;
#pragma unroll
      for (int i = 0; i < 4; ++i) *(LAS float*)(LAS char*)(smem + kvw[i] + ch * KVS) = kacc[i] * ksc;
    }
    WAITL0; BAR;
    if (ch == -1) S2V(2);
    if (ch == 0) S2V(4);
  }
  S2V(5);
  {
    float V[3][11];
#pragma unroll
    for (int ch = 0; ch < 3; ++ch)
#pragma unroll
      for (int x = 0; x < 11; ++x) V[ch][x] = *(const LAS float*)(LAS char*)(smem + gidx[x] + ch * KVS);
    SCHED0;
#pragma unroll
    for (int ch = 0; ch < 3; ++ch) {
      u32 P[11];
#pragma unroll
      for (int x = 1; x < 11; ++x) P[x] = pk_bf16(V[ch][x], V[ch][x - 1]);
#pragma unroll
      for (int sg = 0; sg < 4; ++sg) {
        u32x4 c = {P[sg + 7], P[sg + 5], P[sg + 3], P[sg + 1]};
        *(LAS u32x4*)(LAS char*)(smem + ch * CPY + sg * CPS + tid * 16) = c;
      }
    }
  }
  if (tid < 24) *(LAS u32x4*)(LAS char*)(smem + (tid >> 1) * CPS + 4096 + (tid & 1) * 16) = (u32x4)(0u);
  WAITV0; WAITL0; BAR;
  S2V(6);
  auto finish = [&](const f32x4 s, const u32x2 uu, int ch, int i) {
    const float dskc = ch == 0 ? dsk[0] : ch == 1 ? dsk[1] : dsk[2];
    const int t = 256 * i + 16 * i16 + 4 * q;
    float a0 = rbf(s[0]) + rbf(lo_f(uu[0]) * dskc), a1 = rbf(s[1]) + rbf(hi_f(uu[0]) * dskc);
    float a2 = rbf(s[2]) + rbf(lo_f(uu[1]) * dskc), a3 = rbf(s[3]) + rbf(hi_f(uu[1]) * dskc);
    u32x2 pv = {pk_bf16(a0, a1), pk_bf16(a2, a3)};
    *(LAS u32x2*)(LAS char*)(smem + US + ch * USTR + 480 + t * 2) = pv;
  };
  const u32 abase0 = (i16 & 3) * CPS + (2044 - 16 + 8 * q - 4 * (i16 >> 2)) * 2;
  const u32 bbase0 = US + 480 + (16 * i16 - 16 + 8 * q) * 2;
  if (wave < 3) {
    constexpr int NTL = 8;
    f32x4 Y[NTL];
#pragma unroll
    for (int i = 0; i < NTL; ++i) Y[i] = (f32x4)(0.f);
    const u32 abase = abase0 + wave * CPY, bbase = bbase0 + wave * USTR;
    bf16x8 A0[NTL], B0[NTL], A1[NTL], B1[NTL];
    auto mm = [&](const bf16x8 (&A)[NTL], const bf16x8 (&B)[NTL], bf16x8 (&An)[NTL], bf16x8 (&Bn)[NTL], int cn, bool doit, bool more) {
      const u32 aa = abase - cn * 64, bb = bbase - cn * 64;
      u32x2 lo[NTL], hi[NTL];
      int idx = 0;
      WAITL0; SCHED0;
#pragma unroll
      for (int k = 0; k < NTL; ++k)
#pragma unroll
        for (int pp = 0; pp + k < NTL; ++pp) {
          if (doit) Y[pp + k] = __builtin_amdgcn_mfma_f32_16x16x32_bf16(A[pp], B[k], Y[pp + k], 0, 0, 0);
          if (more) {
            if (idx < NTL) Bn[idx] = *(const LAS bf16x8*)(LAS char*)(smem + bb + idx * 512);
            else if (idx < 2 * NTL) lo[idx - NTL] = *(const LAS u32x2*)(LAS char*)(smem + aa - (idx - NTL) * 512);
            else if (idx < 3 * NTL) hi[idx - 2 * NTL] = *(const LAS u32x2*)(LAS char*)(smem + aa - (idx - 2 * NTL) * 512 + 8);
          }
          ++idx;
          SCHED0;
        }
      if (more) {
#pragma unroll
        for (int pp = 0; pp < NTL; ++pp) An[pp] = __builtin_bit_cast(bf16x8, (u32x4){lo[pp][0], lo[pp][1], hi[pp][0], hi[pp][1]});
      }
    };
    mm(A1, B1, A0, B0, 0, false, true);
    for (int c = 0; c < 8; c += 2) {
      mm(A0, B0, A1, B1, c + 1, true, true);
      mm(A1, B1, A0, B0, c + 2, true, c + 2 < 8);
    }
    S2V(7);
    u32x2 uu[NTL];
#pragma unroll
    for (int i = 0; i < NTL; ++i) uu[i] = *(const LAS u32x2*)(LAS char*)(smem + US + wave * USTR + 480 + (256 * i + 16 * i16 + 4 * q) * 2);
    SCHED0;
#pragma unroll
    for (int i = 0; i < NTL; ++i) finish(Y[i], uu[i], wave, i);
    WAITL0;
    {
      const rsrc_t ra = mk_rsrc(ws + WS_AT + (size_t)(d0 + wave) * 4096, 4096);
#pragma unroll
      for (int k = 0; k < 4; ++k) {
        const u32x4 v = *(const LAS u32x4*)(LAS char*)(smem + US + wave * USTR + 480 + (k * 64 + lane) * 16);
        __builtin_amdgcn_raw_buffer_store_b128(v, ra, (k * 64 + lane) * 16, 0, AUX_SC1);
      }
    }
    S2V(8);
  }
  WAITV0; BAR;
  S2V(10); S2R(15);
  if (tid < 4) __hip_atomic_fetch_add((u32*)(ws + WS_CNT + ((d0 / 72) * 4 + tid) * 256), 1u, __ATOMIC_RELAXED, __HIP_MEMORY_SCOPE_AGENT);
}

__global__ __launch_bounds__(256, 2) void k2_gemm2_conv(const u16* __restrict__ are, const u16* __restrict__ aim,
    const u16* __restrict__ bre, const u16* __restrict__ bim, const u16* __restrict__ cre, const u16* __restrict__ cim,
    const u16* __restrict__ logdt, const u16* __restrict__ dskip, char* __restrict__ ws, u16* __restrict__ out) {
  extern __shared__ __attribute__((aligned(16))) char smem[];
  const int b = blockIdx.x;
  const int wave = __builtin_amdgcn_readfirstlane(threadIdx.x >> 6), lane = threadIdx.x & 63;
  if (b < NCONV) conv_role(smem, are, aim, bre, bim, cre, cim, logdt, dskip, ws, b, wave, lane);
  else { const int g = b - NCONV; gemm2_role(smem, ws, out, g & 7, g >> 3, wave, lane); }
}

extern "C" void kernel_launch(void* const* d_in, const int* in_sizes, int n_in, void* d_out, int out_size, void* d_ws, size_t ws_size, hipStream_t stream) {
  (void)hipFuncSetAttribute((const void*)k1_gemm1, hipFuncAttributeMaxDynamicSharedMemorySize, K1_LDS);
  (void)hipFuncSetAttribute((const void*)k2_gemm2_conv, hipFuncAttributeMaxDynamicSharedMemorySize, K2_LDS);
  k1_gemm1<<<256, 512, K1_LDS, stream>>>((const u16*)d_in[0], (const u16*)d_in[10], (const u16*)d_in[11], (const u16*)d_in[12], (const u16*)d_in[13], (char*)d_ws,
      (const u16*)d_in[1], (const u16*)d_in[2], (const u16*)d_in[3], (const u16*)d_in[4], (const u16*)d_in[5], (const u16*)d_in[6], (const u16*)d_in[7], (const u16*)d_in[8], (const u16*)d_out);
  k2_gemm2_conv<<<NCONV + 256, 256, K2_LDS, stream>>>((const u16*)d_in[1], (const u16*)d_in[2], (const u16*)d_in[3], (const u16*)d_in[4],
      (const u16*)d_in[5], (const u16*)d_in[6], (const u16*)d_in[7], (const u16*)d_in[8], (char*)d_ws, (u16*)d_out);
}
```

```cpp
#include <hip/hip_runtime.h>
typedef unsigned short u16;
typedef unsigned int u32;
typedef short s16x4 __attribute__((ext_vector_type(4)));
typedef __bf16 bf16x8 __attribute__((ext_vector_type(8)));
typedef __bf16 bf16x2 __attribute__((ext_vector_type(2)));
typedef _Float16 f16x2 __attribute__((ext_vector_type(2)));
typedef _Float16 f16x8 __attribute__((ext_vector_type(8)));
typedef float f32x2 __attribute__((ext_vector_type(2)));
typedef float f32x4 __attribute__((ext_vector_type(4)));
typedef float f32x16 __attribute__((ext_vector_type(16)));
typedef u32 u32x2 __attribute__((ext_vector_type(2)));
typedef u32 u32x4 __attribute__((ext_vector_type(4)));
#define LAS __attribute__((address_space(3)))
#define GAS __attribute__((address_space(1)))

constexpr int L_ = 2048, D_ = 576, NS_ = 128, F_ = 4608;
constexpr size_t WS_H   = 0;
constexpr size_t WS_WF  = WS_H + (size_t)32 * 144 * 4096;
constexpr size_t WS_UT  = WS_WF + (size_t)8 * 144 * 4608;
constexpr size_t WS_AT  = WS_UT + (size_t)D_ * L_ * 2;
constexpr size_t WS_G   = WS_AT + (size_t)D_ * L_ * 2;
constexpr size_t WS_CNT = WS_G + (size_t)L_ * 4;
constexpr size_t WS_DBG = 48u << 20;

__device__ __forceinline__ float bf2f(u16 v) { return __uint_as_float(((u32)v) << 16); }
__device__ __forceinline__ u32 pk_bf16(float lo, float hi) {
  f32x2 v = {lo, hi}; bf16x2 b = __builtin_convertvector(v, bf16x2); return __builtin_bit_cast(u32, b);
}
__device__ __forceinline__ float lo_f(u32 p) { return __uint_as_float(p << 16); }
__device__ __forceinline__ float hi_f(u32 p) { return __uint_as_float(p & 0xffff0000u); }
__device__ __forceinline__ void mul_halves8(u32 (&q)[8]) {
  asm("v_dot2_f32_bf16 %0, %0, %8, 0\n\tv_dot2_f32_bf16 %1, %1, %9, 0\n\tv_dot2_f32_bf16 %2, %2, %10, 0\n\tv_dot2_f32_bf16 %3, %3, %11, 0\n\t"
      "v_dot2_f32_bf16 %4, %4, %12, 0\n\tv_dot2_f32_bf16 %5, %5, %13, 0\n\tv_dot2_f32_bf16 %6, %6, %14, 0\n\tv_dot2_f32_bf16 %7, %7, %15, 0\n\ts_nop 2"
      : "+v"(q[0]), "+v"(q[1]), "+v"(q[2]), "+v"(q[3]), "+v"(q[4]), "+v"(q[5]), "+v"(q[6]), "+v"(q[7])
      : "v"(q[0] >> 16), "v"(q[1] >> 16), "v"(q[2] >> 16), "v"(q[3] >> 16), "v"(q[4] >> 16), "v"(q[5] >> 16), "v"(q[6] >> 16), "v"(q[7] >> 16));
}
__device__ __forceinline__ float wave_sum(float v) {
  v += __int_as_float(__builtin_amdgcn_update_dpp(0, __float_as_int(v), 0x111, 0xf, 0xf, true));
  v += __int_as_float(__builtin_amdgcn_update_dpp(0, __float_as_int(v), 0x112, 0xf, 0xf, true));
  v += __int_as_float(__builtin_amdgcn_update_dpp(0, __float_as_int(v), 0x114, 0xf, 0xf, true));
  v += __int_as_float(__builtin_amdgcn_update_dpp(0, __float_as_int(v), 0x118, 0xf, 0xf, true));
  const int iv = __float_as_int(v);
  return (__int_as_float(__builtin_amdgcn_readlane(iv, 15)) + __int_as_float(__builtin_amdgcn_readlane(iv, 31))) +
         (__int_as_float(__builtin_amdgcn_readlane(iv, 47)) + __int_as_float(__builtin_amdgcn_readlane(iv, 63)));
}
__device__ __forceinline__ float rbf(float x) { return lo_f(pk_bf16(x, 0.f)); }

__device__ __forceinline__ void glds16(const void* g, u32 lds_byte, char* smem) {
  __builtin_amdgcn_global_load_lds((const GAS void*)g, (LAS void*)(smem + lds_byte), 16, 0, 0);
}
typedef __amdgpu_buffer_rsrc_t rsrc_t;
__device__ __forceinline__ rsrc_t mk_rsrc(const void* p, u32 bytes) {
  return __builtin_amdgcn_make_buffer_rsrc((void*)p, 0, bytes, 0x00020000);
}
__device__ __forceinline__ void blds16(rsrc_t r, u32 voff, u32 soff, u32 lds_byte, char* smem) {
  __builtin_amdgcn_raw_ptr_buffer_load_lds(r, (LAS void*)(smem + lds_byte), 16, voff, soff, 0, 0);
}
__device__ __forceinline__ s16x4 tr_read(char* smem, u32 off) {
  return __builtin_amdgcn_ds_read_tr16_b64_v4i16((LAS s16x4*)(smem + off));
}
#define WAITV0 asm volatile("s_waitcnt vmcnt(0)" ::: "memory")
#define WAITL0 asm volatile("s_waitcnt lgkmcnt(0)" ::: "memory")
#define BAR __builtin_amdgcn_s_barrier()
#define SCHED0 __builtin_amdgcn_sched_barrier(0)

#define DMA_EVERY 2
#define DMA_N0 4
#define DMA_N1 4
constexpr int X0_ = 0, XST = 32768, W0_ = 65536, WST = 36992, UOFF = 18432 + 128;
constexpr int WDN = X0_ + XST;
constexpr int UTS = W0_ + 2 * WST;
constexpr int GWL = UTS + 9216;
constexpr int K1_LDS = GWL + 1152;

template <int NT>
struct Frag { bf16x8 x[2]; bf16x8 w[NT]; };

template <int NT>
__device__ __forceinline__ void k1_load_frags(Frag<NT>& f, char* smem, u32 xaddr, u32 waddr, int stage, int s) {
#pragma unroll
  for (int xi = 0; xi < 2; ++xi)
    f.x[xi] = *(const LAS bf16x8*)(LAS char*)(smem + xaddr + stage * XST + xi * 4096);
#pragma unroll
  for (int T = 0; T < NT; ++T) {
    s16x4 a = tr_read(smem, waddr + stage * WST + (16 * s) * 288 + 32 * T);
    s16x4 b = tr_read(smem, waddr + stage * WST + (16 * s + 4) * 288 + 32 * T);
    typedef short s16x8 __attribute__((ext_vector_type(8)));
    s16x8 c = __builtin_shufflevector(a, b, 0, 1, 2, 3, 4, 5, 6, 7);
    f.w[T] = __builtin_bit_cast(bf16x8, c);
  }
}
template <int NT>
__device__ __forceinline__ void k1_mma(f32x16 (&acc)[NT][2], const Frag<NT>& f) {
#pragma unroll
  for (int T = 0; T < NT; ++T)
#pragma unroll
    for (int xi = 0; xi < 2; ++xi)
      acc[T][xi] = __builtin_amdgcn_mfma_f32_32x32x16_bf16(f.w[T], f.x[xi], acc[T][xi], 0, 0, 0);
}
template <int NT, int NDMA>
__device__ __forceinline__ void k1_interleave() {
#pragma unroll
  for (int i = 0; i < NT * 2; ++i) {
    __builtin_amdgcn_sched_group_barrier(0x8, 1, 0);
    if (i < 2) __builtin_amdgcn_sched_group_barrier(0x100, 2, 0); else __builtin_amdgcn_sched_group_barrier(0x100, 1, 0);
    if (i < NDMA) __builtin_amdgcn_sched_group_barrier(0x10, 1, 0);
  }
}

template <int NT, int WN>
__device__ __forceinline__ void k1_body(char* smem, const u16* __restrict__ x, const u16* __restrict__ wg, const u16* __restrict__ wu,
                                        const u16* __restrict__ wd, const u16* __restrict__ gw, char* __restrict__ ws, int ib, int jb, int wave, int lane,
                                        const u16* p0, const u16* p1, const u16* p2, const u16* p3, const u16* p4, const u16* p5, const u16* p6, const u16* p7) {
  constexpr int NW = (WN == 0) ? 4 : 5;
  const int wm = wave & 3;
  u32 xoff[4], woff[NW];
#pragma unroll
  for (int ii = 0; ii < 4; ++ii) {
    int rr = lane >> 3, row = 32 * wave + 8 * ii + rr;
    int c = (lane & 7) ^ ((row >> 1) & 7);
    xoff[ii] = row * 1152 + c * 16;
  }
  const int wi0 = (WN == 0) ? 4 * wave : 16 + 5 * (wave - 4);
#pragma unroll
  for (int k = 0; k < NW; ++k) {
    int wi = wi0 + k; int gi = wi >= 18 ? wi - 18 : wi;
    int ci = 64 * gi + lane, row = ci / 18, w18 = ci - row * 18;
    int colb = (w18 < 16) ? (256 * jb + 16 * w18) : (8192 + 32 * jb + 16 * (w18 - 16));
    woff[k] = row * 9216 + colb;
  }
  const rsrc_t rx = mk_rsrc((const char*)x + (size_t)ib * 256 * 1152, 256 * 1152);
  const rsrc_t rg = mk_rsrc(wg, D_ * F_ * 2), ru = mk_rsrc(wu, D_ * F_ * 2);
  const int r32 = lane & 31, hh = lane >> 5;
  u32 xa[4];
#pragma unroll
  for (int s = 0; s < 4; ++s) xa[s] = X0_ + (64 * wm + r32) * 128 + (((2 * s + hh) ^ ((r32 >> 1) & 7)) * 16);
  const int g4 = lane >> 4;
  const u32 wa = W0_ + ((g4 & 1) ? UOFF : 0) + (8 * (g4 >> 1) + ((lane & 15) >> 2)) * 288 + (lane & 3) * 8 + (WN ? 5 * 32 : 0);

  f32x16 acc[NT][2];
#pragma unroll
  for (int T = 0; T < NT; ++T) { acc[T][0] = (f32x16)(0.f); acc[T][1] = (f32x16)(0.f); }
  Frag<NT> f0, f1;
  const int lane_ = lane;

  auto dma1 = [&](int t, int stage, int d) {
    if (d < 4) blds16(rx, xoff[d], t * 128, X0_ + stage * XST + (32 * wave + 8 * d) * 128, smem);
    else {
      const int k = d - 4; int wi = wi0 + k; bool up = wi >= 18; int gi = up ? wi - 18 : wi;
      blds16(up ? ru : rg, woff[k], t * 64 * 9216, W0_ + stage * WST + (up ? UOFF : 0) + gi * 1024, smem);
    }
  };
  float gs0 = 0.f, gs1 = 0.f;
  u32 ux0 = 0, ux1 = 0, ugw = 0;
  const int urow = 8 * jb + 2 * (wave - 4);
  const u32 uaddr = X0_ + urow * 128 + (((lane >> 3) ^ ((urow >> 1) & 7)) << 4) + (lane & 7) * 2;
  auto sub = [&](Frag<NT>& cur, Frag<NT>& nxt, bool do_mma, int stage, int s, int t1, int d0, int nd) {
    typedef short s16x8 __attribute__((ext_vector_type(8)));
    s16x4 ta[NT], tb[NT];
    int di = 0;
#pragma unroll
    for (int i = 0; i < 2 * NT; ++i) {
      if (do_mma) acc[i >> 1][i & 1] = __builtin_amdgcn_mfma_f32_32x32x16_bf16(cur.w[i >> 1], cur.x[i & 1], acc[i >> 1][i & 1], 0, 0, 0);
      if (i == 0) {
        nxt.x[0] = *(const LAS bf16x8*)(LAS char*)(smem + xa[s] + stage * XST);
        nxt.x[1] = *(const LAS bf16x8*)(LAS char*)(smem + xa[s] + stage * XST + 4096);
      } else if (i == 1) {
        ta[0] = tr_read(smem, wa + stage * WST + (16 * s) * 288);
        tb[0] = tr_read(smem, wa + stage * WST + (16 * s + 4) * 288);
      } else {
        const int T = i >> 1;
        if ((i & 1) == 0) ta[T] = tr_read(smem, wa + stage * WST + (16 * s) * 288 + 32 * T);
        else              tb[T] = tr_read(smem, wa + stage * WST + (16 * s + 4) * 288 + 32 * T);
      }
      if (WN == 1) {
        if (s == 0 && i == 7) {
          ux0 = *(const LAS u16*)(LAS char*)(smem + uaddr + stage * XST);
          ux1 = *(const LAS u16*)(LAS char*)(smem + uaddr + stage * XST + 128);
          ugw = *(const LAS u16*)(LAS char*)(smem + GWL + ((t1 - 1) * 64 + lane) * 2);
        }
        if (s == 1 && i == 4) gs0 += rbf(__uint_as_float(ux0 << 16) * __uint_as_float(ugw << 16));
        if (s == 1 && i == 5) gs1 += rbf(__uint_as_float(ux1 << 16) * __uint_as_float(ugw << 16));
        if (s == 1 && i == 6) *(LAS u32*)(LAS char*)(smem + UTS + ((t1 - 1) * 64 + lane) * 16 + (wave - 4) * 4) = ux0 | (ux1 << 16);
      }
      if (di < nd && (i % DMA_EVERY) == (WN ? 1 : 2) % DMA_EVERY) { dma1(t1, stage ^ 1, d0 + di); ++di; }
      SCHED0;
    }
#pragma unroll
    for (int k = 0; k < 9; ++k) if (di < nd) { dma1(t1, stage ^ 1, d0 + di); ++di; }
#pragma unroll
    for (int T = 0; T < NT; ++T) nxt.w[T] = __builtin_bit_cast(bf16x8, __builtin_shufflevector(ta[T], tb[T], 0, 1, 2, 3, 4, 5, 6, 7));
    SCHED0;
  };
  auto wrelay = [&](int T) {
    int lane = lane_; asm volatile("" : "+v"(lane));
    const int g4 = lane >> 4, i16 = lane & 15, hh2 = g4 & 1;
    const int G = 9 * jb + T, qq = 2 * (G & 1) + hh2;
    char* dst0 = ws + WS_WF + (size_t)(ib * 144 + (G >> 1)) * 4608;
#pragma unroll
    for (int r = 0; r < 3; ++r) {
      const int item = 2 * r + (g4 >> 1), m = item < 5 ? item : 4;
      const u32 la = WDN + (16 * T + 4 * hh2 + (i16 >> 2)) * 144 + m * 32 + (i16 & 3) * 8;
      s16x4 a = tr_read(smem, la), b = tr_read(smem, la + 8 * 144);
      typedef short s16x8 __attribute__((ext_vector_type(8)));
      const u32x4 v = __builtin_bit_cast(u32x4, __builtin_shufflevector(a, b, 0, 1, 2, 3, 4, 5, 6, 7));
      char* dst = dst0 + (m < 4 ? m * 1024 + (qq * 16 + i16) * 16 : 4096 + (qq * 8 + i16) * 16);
      if (item < 5 && (m < 4 || i16 < 8)) *(u32x4*)dst = v;
    }
  };
  u32 pfd = 0;
  u32 pfo;
  if (WN == 0) { const int l2 = (ib & 3) * 48 + wave * 12 + lane, row = l2 / 3, c = l2 - 3 * row; pfo = row * 9216 + (c < 2 ? 256 * jb + 128 * c : 8192 + 32 * jb); }
  else pfo = ((jb & 3) * 64 + (wave - 4) * 16 + lane) * 1152;
  const rsrc_t rpf = WN == 0 ? mk_rsrc(ib >= 4 ? wu : wg, D_ * F_ * 2) : rx;
  auto pf = [&](int tp) {
    if (tp <= 8) {
      const u32 so = WN == 0 ? tp * 64 * 9216 : tp * 128;
      if (lane < (WN == 0 ? 12 : 16)) asm volatile("buffer_load_dword %0, %1, %2, %3 offen" : "+v"(pfd) : "v"(pfo), "s"(rpf), "s"(so) : "memory");
    }
  };
  auto wdn_dma = [&](int T) {
    int lane = lane_; asm volatile("" : "+v"(lane));
    const rsrc_t rd = mk_rsrc((const char*)wd + ib * 144, F_ * D_ * 2 - ib * 144);
    const int f0 = T < 8 ? 128 * jb + 16 * T : 4096 + 16 * jb;
#pragma unroll
    for (int r = 0; r < 3; ++r) {
      const int ci = 64 * r + lane;
      if (ci < 144) {
        const int row = ci / 9, cc = ci - 9 * row;
        blds16(rd, (f0 + row) * 1152 + cc * 16, 0, WDN + T * 2304 + r * 1024, smem);
      }
    }
  };
  auto step = [&](int t, int stage, bool first, bool last) {
    {
      int ne = 0;
      ne += first ? 2 : (t - 1 <= 5 ? 1 : 0);
      if (ne == 0) WAITV0;
      else if (ne == 1) asm volatile("s_waitcnt vmcnt(1)" ::: "memory");
      else if (ne == 2) asm volatile("s_waitcnt vmcnt(2)" ::: "memory");
      else if (ne == 3) asm volatile("s_waitcnt vmcnt(3)" ::: "memory");
      else asm volatile("s_waitcnt vmcnt(4)" ::: "memory");
    }
    WAITL0; BAR; SCHED0;
    if (last) { wdn_dma(wave); if (wave == 7) wdn_dma(8); SCHED0; }
    constexpr int ND = 4 + NW;
    constexpr int N0 = DMA_N0, N1 = (ND - N0 < DMA_N1) ? ND - N0 : DMA_N1, N2 = ND - N0 - N1;
    sub(f1, f0, !first, stage, 0, t + 1, 0, last ? 0 : N0);
    sub(f0, f1, true, stage, 1, t + 1, N0, last ? 0 : N1);
    sub(f1, f0, true, stage, 2, t + 1, N0 + N1, last ? 0 : N2);
    SCHED0;
    if (t <= 5) pf(t + 3);
    SCHED0;
    sub(f0, f1, true, stage, 3, t + 1, 0, 0);
  };
  if (wave == 4) {
    const rsrc_t rgw = mk_rsrc(gw, D_ * 2);
    blds16(rgw, lane * 16, 0, GWL, smem);
    if (lane < 8) blds16(rgw, 1024 + lane * 16, 0, GWL + 1024, smem);
  }
#pragma unroll
  for (int p = 0; p < 4 + NW; ++p) dma1(0, 0, p);
pf(1); pf(2);
  step(0, 0, true, false);
  for (int t = 1; t < 7; t += 2) { step(t, 1, false, false); step(t + 1, 0, false, false); }
  step(7, 1, false, false);
  step(8, 0, false, true);
  k1_mma<NT>(acc, f1);
  SCHED0;
  if (WN == 0 && wave == 0) {
    int ln = lane_; asm volatile("" : "+v"(ln));
    const int a = min(ln / 6, 5), c = ln % 6;
    const u16* pa = a == 0 ? p0 : a == 1 ? p1 : a == 2 ? p2 : a == 3 ? p3 : a == 4 ? p4 : p5;
    pa = ln < 36 ? pa + blockIdx.x * 384 + c * 64 : (ln == 36 ? p6 : p7) + (blockIdx.x * 3 & ~1);
    if (blockIdx.x < 192 && ln < 38) asm volatile("global_load_dword %0, %1, off" : "+v"(pfd) : "v"(pa) : "memory");
  }
  if (WN == 1) {
    const float t0 = wave_sum(gs0), t1 = wave_sum(gs1);
    const float v = rbf(lane == 0 ? t0 : t1);
    if (lane < 2) ((float*)(ws + WS_G))[256 * ib + urow + lane] = rbf(1.f / (1.f + expf(-v)));
    if (blockIdx.x == 0 && wave == 4 && lane < 32) *(u32*)(ws + WS_CNT + lane * 256) = 0u;
  }

  char* hb = ws + WS_H + (size_t)(4 * ib + wm) * 144 * 4096 + ((r32 >> 4) * 1024 + (hh * 16 + (r32 & 15)) * 16);
#pragma unroll
  for (int T = 0; T < NT; ++T) {
    const int G = 9 * jb + T + (WN ? 5 : 0);
#pragma unroll
    for (int xi = 0; xi < 2; ++xi) {
      u32x4 o;
      u32 qq[8];
#pragma unroll
      for (int i = 0; i < 8; ++i) qq[i] = pk_bf16(acc[T][xi][i], acc[T][xi][8 + i]);
      mul_halves8(qq);
#pragma unroll
      for (int i = 0; i < 4; ++i) o[i] = pk_bf16(__uint_as_float(qq[2 * i]), __uint_as_float(qq[2 * i + 1]));
      *(u32x4*)(hb + (size_t)(G >> 1) * 4096 + xi * 2048 + (G & 1) * 512) = o;
    }
  }
  WAITL0; BAR; SCHED0;
  {
    const int tid = wave * 64 + lane;
    char* ut = ws + WS_UT + (256 * ib + 8 * jb) * 2;
    *(u32x4*)(ut + (size_t)tid * 4096) = *(const LAS u32x4*)(LAS char*)(smem + UTS + tid * 16);
    if (tid < 64) *(u32x4*)(ut + (size_t)(512 + tid) * 4096) = *(const LAS u32x4*)(LAS char*)(smem + UTS + (512 + tid) * 16);
  }
  WAITV0; SCHED0;
  wrelay(wave);
  if (wave == 7) wrelay(8);
  WAITV0;
  asm volatile("" :: "v"(pfd));
}

__global__ __launch_bounds__(512, 2) void k1_gemm1(const u16* __restrict__ x, const u16* __restrict__ wg, const u16* __restrict__ wu,
                                                   const u16* __restrict__ wd, const u16* __restrict__ gw, char* __restrict__ ws,
                                                   const u16* p0, const u16* p1, const u16* p2, const u16* p3, const u16* p4, const u16* p5, const u16* p6, const u16* p7) {
  extern __shared__ __attribute__((aligned(16))) char smem[];
  const int b = blockIdx.x, idx = b >> 3;
  const int ib = idx & 7, jb = 4 * (b & 7) + (idx >> 3);
  const int wave = __builtin_amdgcn_readfirstlane(threadIdx.x >> 6), lane = threadIdx.x & 63;
  if (wave < 4) k1_body<5, 0>(smem, x, wg, wu, wd, gw, ws, ib, jb, wave, lane, p0, p1, p2, p3, p4, p5, p6, p7);
  else          k1_body<4, 1>(smem, x, wg, wu, wd, gw, ws, ib, jb, wave, lane, p0, p1, p2, p3, p4, p5, p6, p7);
}

#define S2V(i)
#define S2R(i)
#define S2ID
constexpr int K2_LDS = 72672;
constexpr int NCONV = 192;
constexpr u32 AUX_SC1 = 16;

__device__ __forceinline__ u32x4 bload16(rsrc_t r, u32 voff, u32 soff) {
  return __builtin_bit_cast(u32x4, __builtin_amdgcn_raw_buffer_load_b128(r, voff, soff, 0));
}
__device__ __forceinline__ int gbase(int G) {
  int jb = G / 9, T = G - 9 * jb;
  return T < 8 ? 128 * jb + 16 * T : 4096 + 16 * jb;
}

#define G2D 2
__device__ __forceinline__ void gemm2_role(char* smem, char* __restrict__ ws, u16* __restrict__ out,
                                           int ib, int idx, int wave, int lane) {
  const int rowsub = idx & 3, cb = idx >> 2, rb64 = 4 * ib + rowsub;
  const rsrc_t rh = mk_rsrc(ws + WS_H + (size_t)rb64 * 144 * 4096, 144 * 4096);
  const rsrc_t rw = mk_rsrc(ws + WS_WF + (size_t)cb * 144 * 4608, 144 * 4608);
  const int q = lane >> 4, i16 = lane & 15;
  f32x4 acc[5][4];
#pragma unroll
  for (int a = 0; a < 5; ++a)
#pragma unroll
    for (int b = 0; b < 4; ++b) acc[a][b] = (f32x4)(0.f);
  u32x4 hb[G2D][4], wb[G2D][5];
  const u32 hv = lane * 16, hv4 = 4096 + (q * 8 + (i16 & 7)) * 16;
  auto ldh = [&](int j, int r, int ks) { hb[j][r] = bload16(rh, hv + r * 1024, ks * 4096); };
  auto ldw = [&](int j, int m, int ks) { wb[j][m] = m < 4 ? bload16(rw, hv + m * 1024, ks * 4608) : bload16(rw, hv4, ks * 4608); };
  auto ldall = [&](int j, int ks) {
#pragma unroll
    for (int r = 0; r < 3; ++r) ldh(j, r, ks);
#pragma unroll
    for (int m = 0; m < 5; ++m) ldw(j, m, ks);
    ldh(j, 3, ks);
    SCHED0;
  };
  auto kstep = [&](int j, int ksn, bool more) {
#pragma unroll
    for (int r = 0; r < 4; ++r) {
#pragma unroll
      for (int m = 0; m < 5; ++m) {
        acc[m][r] = __builtin_amdgcn_mfma_f32_16x16x32_bf16(__builtin_bit_cast(bf16x8, wb[j][m]), __builtin_bit_cast(bf16x8, hb[j][r]), acc[m][r], 0, 0, 0);
        if (r == 3 && more) { SCHED0; ldw(j, m, ksn); SCHED0; }
      }
      SCHED0;
      if (more) ldh(j, r, ksn);
      SCHED0;
    }
  };
  S2V(0); S2R(14); S2ID;
#pragma unroll
  for (int j = 0; j < G2D; ++j) ldall(j, wave + 4 * j);
  SCHED0;
  constexpr int NIT = 36 / G2D;
  const u32* cnt = (const u32*)(ws + WS_CNT + (cb * 4 + rowsub) * 256);
  for (int it = 0; it < NIT - 1; ++it) {
#pragma unroll
    for (int j = 0; j < G2D; ++j) kstep(j, wave + 4 * (G2D * (it + 1) + j), true);
    if (it == 0) S2V(1);
  }
  S2V(2);
  const u32 early = __hip_atomic_load(cnt, __ATOMIC_RELAXED, __HIP_MEMORY_SCOPE_AGENT);
  const int tid = wave * 64 + lane;
  u32x4 av[3];
  const float gv = ((const float*)(ws + WS_G))[rb64 * 64 + 16 * wave + i16];
  const rsrc_t ra = mk_rsrc(ws + WS_AT + (size_t)cb * 72 * 4096 + rb64 * 128, 72 * 4096);
  auto lda = [&]() {
#pragma unroll
    for (int k = 0; k < 3; ++k) {
      const int ci = min(tid + 256 * k, 575);
      av[k] = __builtin_bit_cast(u32x4, __builtin_amdgcn_raw_buffer_load_b128(ra, (ci >> 3) * 4096 + (ci & 7) * 16, 0, AUX_SC1));
    }
  };
  SCHED0;
  S2V(3);
#pragma unroll
  for (int j = 0; j < G2D; ++j) kstep(j, 0, false);
  S2V(4);
  SCHED0;
  const bool ready = __builtin_amdgcn_readfirstlane(early) >= NCONV / 8;
  if (ready) lda();
  SCHED0;

#pragma unroll
  for (int r = 0; r < 4; ++r) {
    if (r != wave) {
      const int slot = (wave - r - 1) & 3;
#pragma unroll
      for (int dt = 0; dt < 5; ++dt)
        *(LAS f32x4*)(LAS char*)(smem + ((r * 3 + slot) * 5 + dt) * 1024 + lane * 16) = acc[dt][r];
    }
  }
  f32x4 own[5];
#pragma unroll
  for (int dt = 0; dt < 5; ++dt) own[dt] = wave == 0 ? acc[dt][0] : wave == 1 ? acc[dt][1] : wave == 2 ? acc[dt][2] : acc[dt][3];
  if (lane == 0) *(LAS u32*)(LAS char*)(smem + 71808 + wave * 4) = ready ? 1u : 0u;
  WAITL0; BAR; asm volatile("" ::: "memory");
  const u32x4 rdy4 = *(const LAS u32x4*)(LAS char*)(smem + 71808);
  const bool allready = __builtin_amdgcn_readfirstlane(rdy4[0] & rdy4[1] & rdy4[2] & rdy4[3]) != 0;
  const float omg = rbf(1.f - gv);
#pragma unroll
  for (int dt = 0; dt < 5; ++dt) {
#pragma unroll
    for (int sl = 0; sl < 3; ++sl) own[dt] += *(const LAS f32x4*)(LAS char*)(smem + ((wave * 3 + sl) * 5 + dt) * 1024 + lane * 16);
#pragma unroll
    for (int i = 0; i < 4; ++i) own[dt][i] = rbf(omg * rbf(own[dt][i]));
  }
  S2V(5);
  if (!allready) {
    if (wave == 0) {
      for (int it = 0; it < (1 << 22); ++it) {
        if (__hip_atomic_load(cnt, __ATOMIC_RELAXED, __HIP_MEMORY_SCOPE_AGENT) >= NCONV / 8) break;
        __builtin_amdgcn_s_sleep(1);
      }
    }
    BAR; asm volatile("" ::: "memory");
    if (!ready) lda();
  }
#pragma unroll
  for (int k = 0; k < 3; ++k) {
    const int ci = tid + 256 * k;
    if (ci < 576) *(LAS u32x4*)(LAS char*)(smem + 61440 + (ci >> 3) * 144 + (ci & 7) * 16) = av[k];
  }
  WAITL0; BAR; asm volatile("" ::: "memory");
  S2V(8);
  u16* orow = out + (size_t)(rb64 * 64 + 16 * wave + i16) * D_ + cb * 72 + 4 * q;
#pragma unroll
  for (int dt = 0; dt < 5; ++dt) {
    if (dt < 4 || q < 2) {
      float o[4];
#pragma unroll
      for (int i = 0; i < 4; ++i) {
        float av = bf2f(*(const LAS u16*)(LAS char*)(smem + 61440 + (16 * dt + 4 * q + i) * 144 + (16 * wave + i16) * 2));
        o[i] = rbf(gv * av) + own[dt][i];
      }
      u32x2 pv = {pk_bf16(o[0], o[1]), pk_bf16(o[2], o[3])};
      *(u32x2*)(orow + 16 * dt) = pv;
    }
  }
  S2V(6);
  S2V(7); S2R(15);
}

constexpr int RS = 528, TABE = 41 * RS, CPS = 4160, CPY = 4 * CPS, KV = 3 * CPY, KVS = 2976, US = KV + 3 * KVS, USTR = 4608;
struct CP { f32x2 v, r; };
__device__ __forceinline__ CP mkcp(f32x2 p) { CP c; c.v = p; c.r = (f32x2){-p.y, p.x}; return c; }
__device__ __forceinline__ f32x2 cmul(f32x2 a, const CP& p) {
  return __builtin_elementwise_fma(p.r, (f32x2){a.y, a.y}, p.v * (f32x2){a.x, a.x});
}
__device__ __forceinline__ f32x2 cexp_(float ar, float air) {
  float e = __builtin_amdgcn_exp2f(ar * 1.44269504088896341f);
  float fr = __builtin_amdgcn_fractf(air);
  return (f32x2){e * __builtin_amdgcn_cosf(fr), e * __builtin_amdgcn_sinf(fr)};
}
__device__ __forceinline__ float wave_max_nonneg(float m) {
  int v = __float_as_int(m);
  v = max(v, __builtin_amdgcn_update_dpp(0, v, 0x111, 0xf, 0xf, true));
  v = max(v, __builtin_amdgcn_update_dpp(0, v, 0x112, 0xf, 0xf, true));
  v = max(v, __builtin_amdgcn_update_dpp(0, v, 0x114, 0xf, 0xf, true));
  v = max(v, __builtin_amdgcn_update_dpp(0, v, 0x118, 0xf, 0xf, true));
  const int a = __builtin_amdgcn_readlane(v, 15), b = __builtin_amdgcn_readlane(v, 31);
  const int c = __builtin_amdgcn_readlane(v, 47), d = __builtin_amdgcn_readlane(v, 63);
  return __int_as_float(max(max(a, b), max(c, d)));
}
__device__ __forceinline__ void st_h2(char* smem, u32 off, f32x2 v) {
  *(LAS f16x2*)(LAS char*)(smem + off) = __builtin_convertvector(v, f16x2);
}

__device__ __forceinline__ void conv_role(char* smem, const u16* __restrict__ are, const u16* __restrict__ aim, const u16* __restrict__ bre,
                                          const u16* __restrict__ bim, const u16* __restrict__ cre, const u16* __restrict__ cim,
                                          const u16* __restrict__ logdt, const u16* __restrict__ dskip, char* __restrict__ ws,
                                          int cw, int wave, int lane) {
  const int tid = wave * 64 + lane, n = tid & 127, half = wave >> 1;
  const int d0 = 3 * cw;
  S2V(0); S2R(14); S2ID;
  u16 raw[3][10], rdt[3], rds[3];
#pragma unroll
  for (int ch = 0; ch < 3; ++ch) {
    const int o = (d0 + ch) * NS_ + n, o2 = o ^ 64;
    rdt[ch] = logdt[d0 + ch]; rds[ch] = dskip[d0 + ch];
    raw[ch][0] = are[o]; raw[ch][1] = aim[o]; raw[ch][2] = bre[o]; raw[ch][3] = bim[o]; raw[ch][4] = cre[o]; raw[ch][5] = cim[o];
    raw[ch][6] = bre[o2]; raw[ch][7] = bim[o2]; raw[ch][8] = cre[o2]; raw[ch][9] = cim[o2];
  }
#pragma unroll
  for (int ch = 0; ch < 3; ++ch)
    glds16(ws + WS_UT + (size_t)(d0 + ch) * 4096 + tid * 16, US + ch * USTR + 480 + wave * 1024, smem);
  SCHED0;
  if (tid < 90) *(LAS u32x4*)(LAS char*)(smem + US + (tid / 30) * USTR + (tid % 30) * 16) = (u32x4)(0u);
  u32 gidx[11];
  {
    const int J = 2044 - 8 * tid;
#pragma unroll
    for (int x = -7; x <= 3; ++x) {
      int j = J + x;
      u32 bits = pk_bf16((float)j, 0.f) & 0xffffu;
      int idx = j < 256 ? j : (int)bits - 17024;
      gidx[x + 7] = KV + (j < 0 ? 740 : idx + (idx >> 3)) * 4;
    }
  }
  const int q = lane >> 4, i16 = lane & 15;
  u32 erow;
  {
    int s = wave, b = i16;
#pragma unroll
    for (int k = 0; k < 3; ++k) if (s > 0 && b < 8) { --s; b *= 2; }
    erow = TABE + (s == 0 ? b : 8 * s + b) * RS + q * 16;
  }
  SCHED0;
  float par[3][4], dsk[3], wsc[3];
#pragma unroll
  for (int ch = 0; ch < 3; ++ch) {
    const float dt = rbf(expf(bf2f(rdt[ch])));
    dsk[ch] = bf2f(rds[ch]);
    const float ar = dt * bf2f(raw[ch][0]), ai = dt * bf2f(raw[ch][1]) * 0.15915494309189535f;
    float br = dt * bf2f(raw[ch][2]), bi = dt * bf2f(raw[ch][3]), cr = bf2f(raw[ch][4]), ci = bf2f(raw[ch][5]);
    const float wr = __builtin_fmaf(cr, br, -(ci * bi)), wi = __builtin_fmaf(cr, bi, ci * br);
    br = dt * bf2f(raw[ch][6]); bi = dt * bf2f(raw[ch][7]); cr = bf2f(raw[ch][8]); ci = bf2f(raw[ch][9]);
    const float wr2 = __builtin_fmaf(cr, br, -(ci * bi)), wi2 = __builtin_fmaf(cr, bi, ci * br);
    float m = fmaxf(fmaxf(fabsf(wr), fabsf(wi)), fmaxf(fabsf(wr2), fabsf(wi2)));
    m = wave_max_nonneg(m);
    int e = m > 0.f ? __builtin_amdgcn_frexp_expf(m) : 14;
    e = e < -100 ? -100 : e;
    wsc[ch] = __builtin_ldexpf(1.f, e - 14);
    const float sc = __builtin_ldexpf(1.f, 14 - e);
    par[ch][0] = half ? ar * 16.f : ar; par[ch][1] = half ? ai * -16.f : ai;
    par[ch][2] = half ? wr * sc : 1.f; par[ch][3] = half ? -wi * sc : 0.f;
  }
  S2V(1);
  const u32 tabw = (half ? 0 : TABE) + n * 4;
  const u32 xrow = ((wave == 0 ? 0 : 8 + 8 * wave) + i16) * RS + q * 16;
  u32 kvw[4];
  {
    const int nrow = wave == 0 ? 16 : wave == 3 ? 9 : 8, r0 = wave == 0 ? 0 : 8 + 8 * wave;
#pragma unroll
    for (int i = 0; i < 4; ++i) kvw[i] = KV + (4 * q + i < nrow ? (r0 + 4 * q + i) * 18 + i16 + (i16 >> 3) : 741) * 4;
  }
  if (tid < 3) *(LAS float*)(LAS char*)(smem + KV + tid * KVS + 740 * 4) = 0.f;
  for (int ch = -1; ch < 3; ++ch) {
    f32x4 kacc = (f32x4)(0.f), kacc2 = (f32x4)(0.f);
    if (ch >= 0) {
      f16x8 ka[8], kb_[8];
#pragma unroll
      for (int kb = 0; kb < 8; ++kb) {
        ka[kb] = *(const LAS f16x8*)(LAS char*)(smem + xrow + kb * 64);
        kb_[kb] = *(const LAS f16x8*)(LAS char*)(smem + erow + kb * 64);
      }
      WAITL0; BAR;
      if (ch == 0) S2V(3);
#pragma unroll
      for (int kb = 0; kb < 8; kb += 2) {
        kacc = __builtin_amdgcn_mfma_f32_16x16x32_f16(ka[kb], kb_[kb], kacc, 0, 0, 0);
        kacc2 = __builtin_amdgcn_mfma_f32_16x16x32_f16(ka[kb + 1], kb_[kb + 1], kacc2, 0, 0, 0);
      }
      SCHED0;
    }
    if (ch < 2) {
      const int c1 = ch + 1;
      const float ar = c1 == 0 ? par[0][0] : c1 == 1 ? par[1][0] : par[2][0];
      const float ai = c1 == 0 ? par[0][1] : c1 == 1 ? par[1][1] : par[2][1];
      const f32x2 W = {c1 == 0 ? par[0][2] : c1 == 1 ? par[1][2] : par[2][2], c1 == 0 ? par[0][3] : c1 == 1 ? par[1][3] : par[2][3]};
      CP P[8];
      P[0] = mkcp(cexp_(ar, ai));
#pragma unroll
      for (int i = 1; i < 4; ++i) P[i] = mkcp(cmul(P[i - 1].v, P[i - 1]));
      P[4] = mkcp(cexp_(ar * 16.f, ai * 16.f));
#pragma unroll
      for (int i = 5; i < 8; ++i) P[i] = mkcp(cmul(P[i - 1].v, P[i - 1]));
      f32x2 X[16];
      X[0] = W; X[1] = cmul(W, P[0]); X[2] = cmul(W, P[1]); X[3] = cmul(X[1], P[1]);
#pragma unroll
      for (int k = 0; k < 4; ++k) X[4 + k] = cmul(X[k], P[2]);
#pragma unroll
      for (int k = 0; k < 8; ++k) X[8 + k] = cmul(X[k], P[3]);
#pragma unroll
      for (int a = 0; a < 16; ++a) st_h2(smem, tabw + a * RS, X[a]);
#pragma unroll
      for (int s = 1; s < 4; ++s) {
        f32x2 Y[8];
        Y[0] = cmul(W, P[s + 3]); Y[1] = cmul(Y[0], P[s]); Y[2] = cmul(Y[0], P[s + 1]); Y[3] = cmul(Y[1], P[s + 1]);
#pragma unroll
        for (int k = 0; k < 4; ++k) Y[4 + k] = cmul(Y[k], P[s + 2]);
#pragma unroll
        for (int a = 0; a < 8; ++a) st_h2(smem, tabw + (16 + 8 * (s - 1) + a) * RS, Y[a]);
      }
      st_h2(smem, tabw + 40 * RS, cmul(W, P[7]));
    }
    if (ch >= 0) {
      SCHED0;
      const float ksc = ch == 0 ? wsc[0] : ch == 1 ? wsc[1] : wsc[2];
      kacc += kacc2;
#pragma unroll
      for (int i = 0; i < 4; ++i) *(LAS float*)(LAS char*)(smem + kvw[i] + ch * KVS) = kacc[i] * ksc;
    }
    WAITL0; BAR;
    if (ch == -1) S2V(2);
    if (ch == 0) S2V(4);
  }
  S2V(5);
  {
    float V[3][11];
#pragma unroll
    for (int ch = 0; ch < 3; ++ch)
#pragma unroll
      for (int x = 0; x < 11; ++x) V[ch][x] = *(const LAS float*)(LAS char*)(smem + gidx[x] + ch * KVS);
    SCHED0;
#pragma unroll
    for (int ch = 0; ch < 3; ++ch) {
      u32 P[11];
#pragma unroll
      for (int x = 1; x < 11; ++x) P[x] = pk_bf16(V[ch][x], V[ch][x - 1]);
#pragma unroll
      for (int sg = 0; sg < 4; ++sg) {
        u32x4 c = {P[sg + 7], P[sg + 5], P[sg + 3], P[sg + 1]};
        *(LAS u32x4*)(LAS char*)(smem + ch * CPY + sg * CPS + tid * 16) = c;
      }
    }
  }
  if (tid < 24) *(LAS u32x4*)(LAS char*)(smem + (tid >> 1) * CPS + 4096 + (tid & 1) * 16) = (u32x4)(0u);
  WAITV0; WAITL0; BAR;
  S2V(6);
  auto finish = [&](const f32x4 s, const u32x2 uu, int ch, int i) {
    const float dskc = ch == 0 ? dsk[0] : ch == 1 ? dsk[1] : dsk[2];
    const int t = 256 * i + 16 * i16 + 4 * q;
    float a0 = rbf(s[0]) + rbf(lo_f(uu[0]) * dskc), a1 = rbf(s[1]) + rbf(hi_f(uu[0]) * dskc);
    float a2 = rbf(s[2]) + rbf(lo_f(uu[1]) * dskc), a3 = rbf(s[3]) + rbf(hi_f(uu[1]) * dskc);
    u32x2 pv = {pk_bf16(a0, a1), pk_bf16(a2, a3)};
    *(LAS u32x2*)(LAS char*)(smem + US + ch * USTR + 480 + t * 2) = pv;
  };
  const u32 abase0 = (i16 & 3) * CPS + (2044 - 16 + 8 * q - 4 * (i16 >> 2)) * 2;
  const u32 bbase0 = US + 480 + (16 * i16 - 16 + 8 * q) * 2;
  if (wave < 3) {
    constexpr int NTL = 8;
    f32x4 Y[NTL];
#pragma unroll
    for (int i = 0; i < NTL; ++i) Y[i] = (f32x4)(0.f);
    const u32 abase = abase0 + wave * CPY, bbase = bbase0 + wave * USTR;
    bf16x8 A0[NTL], B0[NTL], A1[NTL], B1[NTL];
    auto mm = [&](const bf16x8 (&A)[NTL], const bf16x8 (&B)[NTL], bf16x8 (&An)[NTL], bf16x8 (&Bn)[NTL], int cn, bool doit, bool more) {
      const u32 aa = abase - cn * 64, bb = bbase - cn * 64;
      u32x2 lo[NTL], hi[NTL];
      int idx = 0;
      WAITL0; SCHED0;
#pragma unroll
      for (int k = 0; k < NTL; ++k)
#pragma unroll
        for (int pp = 0; pp + k < NTL; ++pp) {
          if (doit) Y[pp + k] = __builtin_amdgcn_mfma_f32_16x16x32_bf16(A[pp], B[k], Y[pp + k], 0, 0, 0);
          if (more) {
            if (idx < NTL) Bn[idx] = *(const LAS bf16x8*)(LAS char*)(smem + bb + idx * 512);
            else if (idx < 2 * NTL) lo[idx - NTL] = *(const LAS u32x2*)(LAS char*)(smem + aa - (idx - NTL) * 512);
            else if (idx < 3 * NTL) hi[idx - 2 * NTL] = *(const LAS u32x2*)(LAS char*)(smem + aa - (idx - 2 * NTL) * 512 + 8);
          }
          ++idx;
          SCHED0;
        }
      if (more) {
#pragma unroll
        for (int pp = 0; pp < NTL; ++pp) An[pp] = __builtin_bit_cast(bf16x8, (u32x4){lo[pp][0], lo[pp][1], hi[pp][0], hi[pp][1]});
      }
    };
    mm(A1, B1, A0, B0, 0, false, true);
    for (int c = 0; c < 8; c += 2) {
      mm(A0, B0, A1, B1, c + 1, true, true);
      mm(A1, B1, A0, B0, c + 2, true, c + 2 < 8);
    }
    S2V(7);
    u32x2 uu[NTL];
#pragma unroll
    for (int i = 0; i < NTL; ++i) uu[i] = *(const LAS u32x2*)(LAS char*)(smem + US + wave * USTR + 480 + (256 * i + 16 * i16 + 4 * q) * 2);
    SCHED0;
#pragma unroll
    for (int i = 0; i < NTL; ++i) finish(Y[i], uu[i], wave, i);
    WAITL0;
    {
      const rsrc_t ra = mk_rsrc(ws + WS_AT + (size_t)(d0 + wave) * 4096, 4096);
#pragma unroll
      for (int k = 0; k < 4; ++k) {
        const u32x4 v = *(const LAS u32x4*)(LAS char*)(smem + US + wave * USTR + 480 + (k * 64 + lane) * 16);
        __builtin_amdgcn_raw_buffer_store_b128(v, ra, (k * 64 + lane) * 16, 0, AUX_SC1);
      }
    }
    S2V(8);
  }
  WAITV0; BAR;
  S2V(10); S2R(15);
  if (tid < 4) __hip_atomic_fetch_add((u32*)(ws + WS_CNT + ((d0 / 72) * 4 + tid) * 256), 1u, __ATOMIC_RELAXED, __HIP_MEMORY_SCOPE_AGENT);
}

__global__ __launch_bounds__(256, 2) void k2_gemm2_conv(const u16* __restrict__ are, const u16* __restrict__ aim,
    const u16* __restrict__ bre, const u16* __restrict__ bim, const u16* __restrict__ cre, const u16* __restrict__ cim,
    const u16* __restrict__ logdt, const u16* __restrict__ dskip, char* __restrict__ ws, u16* __restrict__ out) {
  extern __shared__ __attribute__((aligned(16))) char smem[];
  const int b = blockIdx.x;
  const int wave = __builtin_amdgcn_readfirstlane(threadIdx.x >> 6), lane = threadIdx.x & 63;
  if (b < NCONV) conv_role(smem, are, aim, bre, bim, cre, cim, logdt, dskip, ws, b, wave, lane);
  else { const int g = b - NCONV; gemm2_role(smem, ws, out, g & 7, g >> 3, wave, lane); }
}

extern "C" void kernel_launch(void* const* d_in, const int* in_sizes, int n_in, void* d_out, int out_size, void* d_ws, size_t ws_size, hipStream_t stream) {
  (void)hipFuncSetAttribute((const void*)k1_gemm1, hipFuncAttributeMaxDynamicSharedMemorySize, K1_LDS);
  (void)hipFuncSetAttribute((const void*)k2_gemm2_conv, hipFuncAttributeMaxDynamicSharedMemorySize, K2_LDS);
  k1_gemm1<<<256, 512, K1_LDS, stream>>>((const u16*)d_in[0], (const u16*)d_in[10], (const u16*)d_in[11], (const u16*)d_in[12], (const u16*)d_in[13], (char*)d_ws,
      (const u16*)d_in[1], (const u16*)d_in[2], (const u16*)d_in[3], (const u16*)d_in[4], (const u16*)d_in[5], (const u16*)d_in[6], (const u16*)d_in[7], (const u16*)d_in[8]);
  k2_gemm2_conv<<<NCONV + 256, 256, K2_LDS, stream>>>((const u16*)d_in[1], (const u16*)d_in[2], (const u16*)d_in[3], (const u16*)d_in[4],
      (const u16*)d_in[5], (const u16*)d_in[6], (const u16*)d_in[7], (const u16*)d_in[8], (char*)d_ws, (u16*)d_out);
}
```
